```python
import torch

def _softmax(x, axis=-1):
    return torch.softmax(x, dim=axis)
def _gelu(x):
    return torch.nn.functional.gelu(x, approximate="tanh")
def _silu(x):
    return torch.nn.functional.silu(x)
def _sigmoid(x):
    return torch.sigmoid(x)
def _layernorm(x, axis=-1, eps=1e-5):
    m = x.mean(dim=axis, keepdim=True)
    v = x.var(dim=axis, keepdim=True, unbiased=False)
    return (x - m) / torch.sqrt(v + eps)
def _scatter_add(tgt, idx, src, axis=0):
    return tgt.clone().index_add_(axis, idx, src)
def _segment_sum(src, seg, n):
    out = torch.zeros((n,) + src.shape[1:], dtype=src.dtype)
    return out.index_add_(0, seg, src)
def _pad(x, pw):
    flat = tuple(v for pair in reversed(pw) for v in pair)
    return torch.nn.functional.pad(x, flat)

kernel_name = 'concat_heads__gqa_block_cached__gqa_block_cached__decode'

def setup_inputs():

    _w = (lambda a: (lambda t: t.to(torch.bfloat16) if t.is_floating_point() else t)(torch.as_tensor(a)))
    def _A_setup():
        B, Sq, D = 2, 1, 640
        Hq, Hkv, Sk, Dh = 32, 32, 4096, 128
        assert Hq % Hkv == 0, (Hq, Hkv)
        return {
            "x": _w((lambda *s: torch.randn(*s, dtype=torch.bfloat16) * 0.8)(B, Sq, D)),
            "k_cache": _w((lambda *s: torch.randn(*s, dtype=torch.bfloat16) * 0.8)(B, Hkv, Sk, Dh)),
            "v_cache": _w((lambda *s: torch.randn(*s, dtype=torch.bfloat16) * 0.05)(B, Hkv, Sk, Dh)),
            "wq": _w((lambda *s: torch.randn(*s, dtype=torch.bfloat16) * 0.8)(D, Hq * Dh) * (D ** -0.5)),
            "wo": _w((lambda *s: torch.randn(*s, dtype=torch.bfloat16) * 0.05)(Hq * Dh, D) * ((Hq * Dh) ** -0.5)),
        }
    _A_in = _A_setup()

    def _B_setup():
        B, Sq, D = 2, 1, 640
        Hq, Hkv, Sk, Dh = 32, 32, 4096, 128
        assert Hq % Hkv == 0, (Hq, Hkv)
        return {
            "x": _w((lambda *s: torch.randn(*s, dtype=torch.bfloat16) * 0.8)(B, Sq, D)),
            "k_cache": _w((lambda *s: torch.randn(*s, dtype=torch.bfloat16) * 0.8)(B, Hkv, Sk, Dh)),
            "v_cache": _w((lambda *s: torch.randn(*s, dtype=torch.bfloat16) * 0.05)(B, Hkv, Sk, Dh)),
            "wq": _w((lambda *s: torch.randn(*s, dtype=torch.bfloat16) * 0.8)(D, Hq * Dh) * (D ** -0.5)),
            "wo": _w((lambda *s: torch.randn(*s, dtype=torch.bfloat16) * 0.05)(Hq * Dh, D) * ((Hq * Dh) ** -0.5)),
        }
    _B_in = _B_setup()

    return {
        "A_x": _A_in["x"],
        "A_k_cache": _A_in["k_cache"],
        "A_v_cache": _A_in["v_cache"],
        "A_wq": _A_in["wq"],
        "A_wo": _A_in["wo"],
        "B_x": _B_in["x"],
        "B_k_cache": _B_in["k_cache"],
        "B_v_cache": _B_in["v_cache"],
        "B_wq": _B_in["wq"],
        "B_wo": _B_in["wo"],
    }

def reference(A_x, A_k_cache, A_v_cache, A_wq, A_wo, B_x, B_k_cache, B_v_cache, B_wq, B_wo):
    def _A_ref(x, k_cache, v_cache, wq, wo):
        B, Sq, D = x.shape
        Hq, Dh = 32, 128
        Hkv = k_cache.shape[1]
        rep = Hq // Hkv
        q = (lambda x, *d: x.permute(*d))((x @ wq).reshape(B, Sq, Hq, Dh), 0, 2, 1, 3)
        k = (lambda x, n, axis: torch.repeat_interleave(x, n, dim=axis))(k_cache, rep, axis=1)
        v = (lambda x, n, axis: torch.repeat_interleave(x, n, dim=axis))(v_cache, rep, axis=1)
        scores = torch.einsum("bhqd,bhkd->bhqk", q, k) * (Dh ** -0.5)
        p = _softmax(scores, axis=-1)
        o = torch.einsum("bhqk,bhkd->bhqd", p, v)
        return (lambda x, *d: x.permute(*d))(o, 0, 2, 1, 3).reshape(B, Sq, Hq * Dh) @ wo

    def _B_ref(x, k_cache, v_cache, wq, wo):
        B, Sq, D = x.shape
        Hq, Dh = 32, 128
        Hkv = k_cache.shape[1]
        rep = Hq // Hkv
        q = (lambda x, *d: x.permute(*d))((x @ wq).reshape(B, Sq, Hq, Dh), 0, 2, 1, 3)
        k = (lambda x, n, axis: torch.repeat_interleave(x, n, dim=axis))(k_cache, rep, axis=1)
        v = (lambda x, n, axis: torch.repeat_interleave(x, n, dim=axis))(v_cache, rep, axis=1)
        scores = torch.einsum("bhqd,bhkd->bhqk", q, k) * (Dh ** -0.5)
        p = _softmax(scores, axis=-1)
        o = torch.einsum("bhqk,bhkd->bhqd", p, v)
        return (lambda x, *d: x.permute(*d))(o, 0, 2, 1, 3).reshape(B, Sq, Hq * Dh) @ wo

    a = _A_ref(A_x, A_k_cache, A_v_cache, A_wq, A_wo)
    b = _B_ref(B_x, B_k_cache, B_v_cache, B_wq, B_wo)
    return torch.concatenate([a, b], axis=-1)
```

```cpp
#include <hip/hip_runtime.h>
#include <hip/hip_bf16.h>
#include <stdint.h>

typedef unsigned u32x4 __attribute__((ext_vector_type(4)));
typedef float f32x4 __attribute__((ext_vector_type(4)));
typedef __attribute__((ext_vector_type(2))) __bf16 bf16x2;

constexpr int NB = 2, DM = 640, NH = 32, SK = 4096, DH = 128;
constexpr int NSPLIT = 4;
constexpr int KEYS_PER_SPLIT = SK / NSPLIT;

__device__ __forceinline__ float dot2(unsigned k, unsigned q, float acc) {
  return __builtin_amdgcn_fdot2_f32_bf16(__builtin_bit_cast(bf16x2, k), __builtin_bit_cast(bf16x2, q), acc, false);
}
template <int CTRL> __device__ __forceinline__ float dpp(float x) {
  return __builtin_bit_cast(float, __builtin_amdgcn_mov_dpp(__builtin_bit_cast(int, x), CTRL, 0xf, 0xf, true));
}
__device__ __forceinline__ float bflo(unsigned u) { return __builtin_bit_cast(float, u << 16); }
__device__ __forceinline__ float bfhi(unsigned u) { return __builtin_bit_cast(float, u & 0xffff0000u); }
__device__ __forceinline__ unsigned short f2bf(float f) {
  __hip_bfloat16 h = __float2bfloat16(f);
  return __builtin_bit_cast(unsigned short, h);
}

__global__ void __launch_bounds__(256) qproj_kernel(const unsigned short* xA, const unsigned short* wqA,
                                                    const unsigned short* xB, const unsigned short* wqB,
                                                    unsigned short* q) {
  const int br = blockIdx.x >> 6, cb = blockIdx.x & 63;
  const unsigned short* x = br ? xB : xA;
  const unsigned short* wq = br ? wqB : wqA;
  __shared__ float red[32][2][64];
  const int t = threadIdx.x, cg = t & 7, rg = t >> 3;
  float acc[2][8];
  for (int b = 0; b < 2; ++b) for (int j = 0; j < 8; ++j) acc[b][j] = 0.f;
  for (int i = 0; i < 20; ++i) {
    const int r = rg + 32 * i;
    u32x4 w = *reinterpret_cast<const u32x4*>(wq + (size_t)r * 4096 + cb * 64 + cg * 8);
    float x0 = __builtin_bit_cast(float, (unsigned)x[r] << 16);
    float x1 = __builtin_bit_cast(float, (unsigned)x[DM + r] << 16);
    for (int j = 0; j < 4; ++j) {
      acc[0][2 * j] += x0 * bflo(w[j]); acc[0][2 * j + 1] += x0 * bfhi(w[j]);
      acc[1][2 * j] += x1 * bflo(w[j]); acc[1][2 * j + 1] += x1 * bfhi(w[j]);
    }
  }
  for (int b = 0; b < 2; ++b) for (int j = 0; j < 8; ++j) red[rg][b][cg * 8 + j] = acc[b][j];
  __syncthreads();
  if (t < 128) {
    const int b = t >> 6, c = t & 63;
    float s = 0.f;
    for (int g = 0; g < 32; ++g) s += red[g][b][c];
    q[(br * 2 + b) * 4096 + cb * 64 + c] = f2bf(s);
  }
}

__global__ void __launch_bounds__(256) attn_kernel(const unsigned short* kA, const unsigned short* vA,
                                                   const unsigned short* kB, const unsigned short* vB,
                                                   const unsigned short* q, float* part) {
  const int unit = blockIdx.x / NSPLIT, sp = blockIdx.x % NSPLIT;
  const int br = unit >> 6, bh = unit & 63;
  const unsigned short* K = (br ? kB : kA) + (size_t)bh * SK * DH + (size_t)sp * KEYS_PER_SPLIT * DH;
  const unsigned short* V = (br ? vB : vA) + (size_t)bh * SK * DH + (size_t)sp * KEYS_PER_SPLIT * DH;
  const int t = threadIdx.x, lane = t & 63, wave = t >> 6, g = lane >> 4, c = lane & 15;
  const u32x4 qv = *reinterpret_cast<const u32x4*>(q + (size_t)unit * DH + c * 8);
  constexpr float C2 = 0.08838834764831845f * 1.4426950408889634f;
  float m = -INFINITY, l = 0.f, acc[8];
  for (int j = 0; j < 8; ++j) acc[j] = 0.f;
  constexpr int U = 8;
  constexpr int NBATCH = KEYS_PER_SPLIT / (4 * U * 4);
  for (int i = 0; i < NBATCH; ++i) {
    const size_t base = (size_t)((i * 4 + wave) * (4 * U)) * DH + lane * 8;
    u32x4 kk[U], vv[U];
#pragma unroll
    for (int u = 0; u < U; ++u) kk[u] = __builtin_nontemporal_load(reinterpret_cast<const u32x4*>(K + base + u * 512));
#pragma unroll
    for (int u = 0; u < U; ++u) vv[u] = __builtin_nontemporal_load(reinterpret_cast<const u32x4*>(V + base + u * 512));
    float s[U];
#pragma unroll
    for (int u = 0; u < U; ++u) {
      float d = dot2(kk[u][0], qv[0], 0.f);
      d = dot2(kk[u][1], qv[1], d); d = dot2(kk[u][2], qv[2], d); d = dot2(kk[u][3], qv[3], d);
      d += dpp<0x128>(d); d += dpp<0x124>(d); d += dpp<0x122>(d); d += dpp<0x121>(d);
      s[u] = d * C2;
    }
    float mn = m;
#pragma unroll
    for (int u = 0; u < U; ++u) mn = fmaxf(mn, s[u]);
    const float alpha = __builtin_amdgcn_exp2f(m - mn);
    m = mn; l *= alpha;
#pragma unroll
    for (int j = 0; j < 8; ++j) acc[j] *= alpha;
#pragma unroll
    for (int u = 0; u < U; ++u) {
      const float p = __builtin_amdgcn_exp2f(s[u] - mn);
      l += p;
#pragma unroll
      for (int j = 0; j < 4; ++j) { acc[2 * j] += p * bflo(vv[u][j]); acc[2 * j + 1] += p * bfhi(vv[u][j]); }
    }
  }
  __shared__ float sm[16], sl[16], so[16][128];
  const int sid = wave * 4 + g;
  if (c == 0) { sm[sid] = m; sl[sid] = l; }
  for (int j = 0; j < 8; ++j) so[sid][c * 8 + j] = acc[j];
  __syncthreads();
  if (t < 128) {
    float M = -INFINITY;
    for (int i = 0; i < 16; ++i) M = fmaxf(M, sm[i]);
    float L = 0.f, o = 0.f;
    for (int i = 0; i < 16; ++i) { float w = __builtin_amdgcn_exp2f(sm[i] - M); L += w * sl[i]; o += w * so[i][t]; }
    float* p = part + (size_t)blockIdx.x * 130;
    p[2 + t] = o;
    if (t == 0) { p[0] = M; p[1] = L; }
  }
}

__global__ void __launch_bounds__(256) oproj_kernel(const float* part, const unsigned short* woA, const unsigned short* woB,
                                                    float* y  ) {
  const int br = blockIdx.x >> 5, h = blockIdx.x & 31;
  const unsigned short* wo = (br ? woB : woA) + (size_t)h * DH * DM;
  __shared__ float o[2][128];
  __shared__ float red[3][2][640];
  const int t = threadIdx.x;
  {
    const int b = t >> 7, d = t & 127;
    const int unit = br * 64 + b * 32 + h;
    const float* p = part + (size_t)unit * NSPLIT * 130;
    float M = -INFINITY;
    for (int s = 0; s < NSPLIT; ++s) M = fmaxf(M, p[s * 130]);
    float L = 0.f, ov = 0.f;
    for (int s = 0; s < NSPLIT; ++s) { float w = __builtin_amdgcn_exp2f(p[s * 130] - M); L += w * p[s * 130 + 1]; ov += w * p[s * 130 + 2 + d]; }
    float r = ov / L;
    o[b][d] = __builtin_bit_cast(float, (unsigned)f2bf(r) << 16);
  }
  __syncthreads();
  const int cg = t % 80, rg = t / 80;
  float acc[2][8];
  for (int b = 0; b < 2; ++b) for (int j = 0; j < 8; ++j) acc[b][j] = 0.f;
  if (rg < 3) {
    for (int r = rg; r < 128; r += 3) {
      u32x4 w = *reinterpret_cast<const u32x4*>(wo + (size_t)r * DM + cg * 8);
      const float o0 = o[0][r], o1 = o[1][r];
      for (int j = 0; j < 4; ++j) {
        acc[0][2 * j] += o0 * bflo(w[j]); acc[0][2 * j + 1] += o0 * bfhi(w[j]);
        acc[1][2 * j] += o1 * bflo(w[j]); acc[1][2 * j + 1] += o1 * bfhi(w[j]);
      }
    }
    for (int b = 0; b < 2; ++b) for (int j = 0; j < 8; ++j) red[rg][b][cg * 8 + j] = acc[b][j];
  }
  __syncthreads();
  for (int i = t; i < 1280; i += 256) {
    const int b = i / 640, n = i % 640;
    y[((size_t)(br * 32 + h) * 2 + b) * 640 + n] = red[0][b][n] + red[1][b][n] + red[2][b][n];
  }
}

__global__ void __launch_bounds__(256) final_kernel(const float* y, unsigned short* out) {
  const int i = blockIdx.x * 256 + threadIdx.x;
  if (i >= 2560) return;
  const int b = i / 1280, r = i % 1280, br = r / 640, n = r % 640;
  float s = 0.f;
  for (int h = 0; h < 32; ++h) s += y[((size_t)(br * 32 + h) * 2 + b) * 640 + n];
  out[i] = f2bf(s);
}

extern "C" void kernel_launch(void* const* d_in, const int* in_sizes, int n_in, void* d_out, int out_size, void* d_ws,
                              size_t ws_size, hipStream_t stream) {
  const unsigned short* xA = (const unsigned short*)d_in[0];
  const unsigned short* kA = (const unsigned short*)d_in[1];
  const unsigned short* vA = (const unsigned short*)d_in[2];
  const unsigned short* wqA = (const unsigned short*)d_in[3];
  const unsigned short* woA = (const unsigned short*)d_in[4];
  const unsigned short* xB = (const unsigned short*)d_in[5];
  const unsigned short* kB = (const unsigned short*)d_in[6];
  const unsigned short* vB = (const unsigned short*)d_in[7];
  const unsigned short* wqB = (const unsigned short*)d_in[8];
  const unsigned short* woB = (const unsigned short*)d_in[9];
  unsigned short* q = (unsigned short*)d_ws;
  float* part = (float*)((char*)d_ws + 65536);
  float* y = (float*)((char*)d_ws + 65536 + 1048576);
  qproj_kernel<<<128, 256, 0, stream>>>(xA, wqA, xB, wqB, q);
  attn_kernel<<<128 * NSPLIT, 256, 0, stream>>>(kA, vA, kB, vB, q, part);
  oproj_kernel<<<64, 256, 0, stream>>>(part, woA, woB, y);
  final_kernel<<<10, 256, 0, stream>>>(y, (unsigned short*)d_out);
}
```

```cpp
#include <hip/hip_runtime.h>
#include <hip/hip_bf16.h>
#include <stdint.h>

typedef unsigned u32x4 __attribute__((ext_vector_type(4)));
typedef unsigned u32x2 __attribute__((ext_vector_type(2)));
typedef float f32x4 __attribute__((ext_vector_type(4)));
typedef float f32x2 __attribute__((ext_vector_type(2)));
typedef __attribute__((ext_vector_type(2))) __bf16 bf16x2;
typedef __attribute__((ext_vector_type(8))) __bf16 bf16x8;

#define SB() __builtin_amdgcn_sched_barrier(0)
#define KV_AUX 2
#define W_AUX 2
#define SC1 16

constexpr int NW = 8, H = 8, NTHR = (NW + 1) * 64;
constexpr int NIT = 512 / (NW * H);
constexpr unsigned SPIN_CAP = 1u << 18;

constexpr unsigned WS_EPOCH = 0;
constexpr unsigned WS_QG = 4096;
constexpr unsigned WS_PG = WS_QG + 64 * 8192;
constexpr unsigned WS_YG = WS_PG + 64 * 4 * 2048;
constexpr unsigned WS_END = WS_YG + 160 * 4096;

struct Params {
  const void* x[2]; const void* k[2]; const void* v[2]; const void* wq[2]; const void* wo[2];
  void* out; void* ws;
};

__device__ __forceinline__ float dot2(unsigned k, unsigned q, float acc) {
  return __builtin_amdgcn_fdot2_f32_bf16(__builtin_bit_cast(bf16x2, k), __builtin_bit_cast(bf16x2, q), acc, false);
}
template <int CTRL> __device__ __forceinline__ float dpp(float x) {
  return __builtin_bit_cast(float, __builtin_amdgcn_mov_dpp(__builtin_bit_cast(int, x), CTRL, 0xf, 0xf, true));
}
__device__ __forceinline__ float bflo(unsigned u) { return __builtin_bit_cast(float, u << 16); }
__device__ __forceinline__ float bfhi(unsigned u) { return __builtin_bit_cast(float, u & 0xffff0000u); }
__device__ __forceinline__ unsigned pack_bf16(float lo, float hi) {
  bf16x2 r; r[0] = (__bf16)lo; r[1] = (__bf16)hi;
  return __builtin_bit_cast(unsigned, r);
}
__device__ __forceinline__ float f(unsigned u) { return __builtin_bit_cast(float, u); }
__device__ __forceinline__ unsigned u(float x) { return __builtin_bit_cast(unsigned, x); }
__device__ __forceinline__ float x16_sum(float x) {
  auto s = __builtin_amdgcn_permlane16_swap(u(x), u(x), false, false);
  return f(s[0]) + f(s[1]);
}
__device__ __forceinline__ float x32_sum(float x) {
  auto s = __builtin_amdgcn_permlane32_swap(u(x), u(x), false, false);
  return f(s[0]) + f(s[1]);
}
__device__ __forceinline__ float x16_max(float x) {
  auto s = __builtin_amdgcn_permlane16_swap(u(x), u(x), false, false);
  return fmaxf(f(s[0]), f(s[1]));
}
__device__ __forceinline__ float x32_max(float x) {
  auto s = __builtin_amdgcn_permlane32_swap(u(x), u(x), false, false);
  return fmaxf(f(s[0]), f(s[1]));
}
__device__ __forceinline__ void wg_barrier() {
  asm volatile("s_waitcnt lgkmcnt(0)" ::: "memory");
  __builtin_amdgcn_s_barrier();
  asm volatile("" ::: "memory");
}
__device__ __forceinline__ bf16x8 sel_frag(unsigned val16, const u32x4& mask) {
  const unsigned pp = val16 * 0x10001u;
  u32x4 a = {pp & mask[0], pp & mask[1], pp & mask[2], pp & mask[3]};
  return __builtin_bit_cast(bf16x8, a);
}

#define MK_RSRC(ptr, bytes) __builtin_amdgcn_make_buffer_rsrc((void*)(ptr), 0, (bytes), 0x00020000)

__global__ void __launch_bounds__(NTHR, 1) fused_decode_kernel(Params p) {
  extern __shared__ __attribute__((aligned(16))) unsigned char smem[];
  float* red = reinterpret_cast<float*>(smem);
  float* wml = reinterpret_cast<float*>(smem + 16384);
  unsigned* qs = reinterpret_cast<unsigned*>(smem + 16384 + 64);
  unsigned* os = reinterpret_cast<unsigned*>(smem + 16384 + 64 + 256);

  const int bid = blockIdx.x;
  const int quad = bid >> 2, mem = bid & 3, br = quad >> 5, h = quad & 31, b = mem >> 1, half = mem & 1;
  const int tid = threadIdx.x, lane = tid & 63;
  const int wave = __builtin_amdgcn_readfirstlane(tid >> 6);
  const int g = lane >> 4, c = lane & 15;
  constexpr float C2 = 0.08838834764831845f * 1.4426950408889634f;
  auto rws = MK_RSRC(p.ws, WS_END);

  if (wave < NW) {
    const char* kb = (const char*)p.k[br] + ((size_t)(b * 32 + h) * 2 + half) * (512u << 10);
    const char* vb = (const char*)p.v[br] + ((size_t)(b * 32 + h) * 2 + half) * (512u << 10);
    auto rk = MK_RSRC(kb, 512 << 10);
    auto rv = MK_RSRC(vb, 512 << 10);
    auto rx = MK_RSRC(p.x[br], 2 * 640 * 2);
    auto rwq = MK_RSRC(p.wq[br], 640 * 4096 * 2);
    auto rwo = MK_RSRC(p.wo[br], 4096 * 640 * 2);
    u32x4 mask;
#pragma unroll
    for (int t = 0; t < 4; ++t) mask[t] = (((c & 7) >> 1) == t) ? ((c & 1) ? 0xffff0000u : 0x0000ffffu) : 0u;

    const int r0 = mem * 160;
    const int xoff = (c >> 3) * 1280 + (r0 + 16 * wave + 4 * g) * 2;
    u32x2 xa = __builtin_amdgcn_raw_buffer_load_b64(rx, xoff, 0, 0);
    unsigned xb = __builtin_amdgcn_raw_buffer_load_b16(rx, (c >> 3) * 1280 + (r0 + 128 + 4 * wave + g) * 2, 0, 0);
    SB();
    u32x4 wq[5];
    {
      const int wo_ = (r0 + 16 * wave + 4 * g) * 8192 + h * 256 + c * 16;
#pragma unroll
      for (int i = 0; i < 4; ++i) { wq[i] = __builtin_amdgcn_raw_buffer_load_b128(rwq, wo_ + i * 8192, 0, W_AUX); SB(); }
      wq[4] = __builtin_amdgcn_raw_buffer_load_b128(rwq, (r0 + 128 + 4 * wave + g) * 8192 + h * 256 + c * 16, 0, W_AUX);
      SB();
    }
    u32x4 kr[H], vr[H];
    const int lo = lane * 16;
    int so = wave * (H * 1024);
#pragma unroll
    for (int t = 0; t < H; ++t) { kr[t] = __builtin_amdgcn_raw_buffer_load_b128(rk, lo + t * 1024, so, KV_AUX); SB(); }
#pragma unroll
    for (int t = 0; t < H; ++t) { vr[t] = __builtin_amdgcn_raw_buffer_load_b128(rv, lo + t * 1024, so, KV_AUX); SB(); }
    {
      f32x4 qa = {0.f, 0.f, 0.f, 0.f};
      qa = __builtin_amdgcn_mfma_f32_16x16x32_bf16(sel_frag(xa[0] & 0xffffu, mask), __builtin_bit_cast(bf16x8, wq[0]), qa, 0, 0, 0);
      qa = __builtin_amdgcn_mfma_f32_16x16x32_bf16(sel_frag(xa[0] >> 16, mask), __builtin_bit_cast(bf16x8, wq[1]), qa, 0, 0, 0);
      qa = __builtin_amdgcn_mfma_f32_16x16x32_bf16(sel_frag(xa[1] & 0xffffu, mask), __builtin_bit_cast(bf16x8, wq[2]), qa, 0, 0, 0);
      qa = __builtin_amdgcn_mfma_f32_16x16x32_bf16(sel_frag(xa[1] >> 16, mask), __builtin_bit_cast(bf16x8, wq[3]), qa, 0, 0, 0);
      qa = __builtin_amdgcn_mfma_f32_16x16x32_bf16(sel_frag(xb & 0xffffu, mask), __builtin_bit_cast(bf16x8, wq[4]), qa, 0, 0, 0);
      *reinterpret_cast<f32x4*>(red + wave * 256 + (g >> 1) * 128 + 8 * c + 4 * (g & 1)) = qa;
    }
    wg_barrier();
    wg_barrier();
    const u32x4 q = *reinterpret_cast<const u32x4*>(qs + 4 * c);

    float m = -INFINITY, l = 0.f;
    f32x2 acc[4];
#pragma unroll
    for (int j = 0; j < 4; ++j) acc[j] = f32x2{0.f, 0.f};
    u32x4 wo[8];
    const int wo_off = (h * 128 + 16 * wave + 4 * g) * 1280 + c * 16;

    auto step = [&](auto last_tag, int nso) {
      constexpr bool LAST = decltype(last_tag)::value;
      float s[H];
#pragma unroll
      for (int t = 0; t < H; ++t) {
        float d0 = dot2(kr[t][0], q[0], 0.f), d1 = dot2(kr[t][1], q[1], 0.f);
        d0 = dot2(kr[t][2], q[2], d0); d1 = dot2(kr[t][3], q[3], d1);
        s[t] = d0 + d1;
        SB();
        if constexpr (!LAST) kr[t] = __builtin_amdgcn_raw_buffer_load_b128(rk, lo + t * 1024, nso, KV_AUX);
        else {
          if (t < 4) wo[t] = __builtin_amdgcn_raw_buffer_load_b128(rwo, wo_off + t * 1280, mem * 256, W_AUX);
          else if (mem == 0) wo[t] = __builtin_amdgcn_raw_buffer_load_b128(rwo, wo_off + (t - 4) * 1280, 1024, W_AUX);
        }
        SB();
      }
#pragma unroll
      for (int t = 0; t < H; ++t) s[t] += dpp<0x128>(s[t]);
#pragma unroll
      for (int t = 0; t < H; ++t) s[t] += dpp<0x124>(s[t]);
#pragma unroll
      for (int t = 0; t < H; ++t) s[t] += dpp<0x122>(s[t]);
#pragma unroll
      for (int t = 0; t < H; ++t) s[t] += dpp<0x121>(s[t]);
      float mn = m;
#pragma unroll
      for (int t = 0; t < H; ++t) mn = fmaxf(mn, s[t]);
      const float mc = mn * C2;
      const float alpha = __builtin_amdgcn_exp2f(m * C2 - mc);
      m = mn;
      float ps = 0.f;
#pragma unroll
      for (int t = 0; t < H; ++t) { s[t] = __builtin_amdgcn_exp2f(__builtin_fmaf(s[t], C2, -mc)); ps += s[t]; }
      l = __builtin_fmaf(l, alpha, ps);
#pragma unroll
      for (int j = 0; j < 4; ++j) acc[j] *= alpha;
      SB();
#pragma unroll
      for (int t = 0; t < H; ++t) {
        const f32x2 pp = {s[t], s[t]};
#pragma unroll
        for (int j = 0; j < 4; ++j) acc[j] = __builtin_elementwise_fma(f32x2{bflo(vr[t][j]), bfhi(vr[t][j])}, pp, acc[j]);
        SB();
        if constexpr (!LAST) vr[t] = __builtin_amdgcn_raw_buffer_load_b128(rv, lo + t * 1024, nso, KV_AUX);
        SB();
      }
    };
    for (int it = 1; it < NIT; ++it) {
      so += NW * H * 1024;
      step(std::false_type{}, so);
    }
    step(std::true_type{}, 0);

    {
      const float mw = x32_max(x16_max(m));
      const float sc = __builtin_amdgcn_exp2f((m - mw) * C2);
      l = x32_sum(x16_sum(l * sc));
      float a[8];
#pragma unroll
      for (int j = 0; j < 4; ++j) { a[2 * j] = x32_sum(x16_sum(acc[j][0] * sc)); a[2 * j + 1] = x32_sum(x16_sum(acc[j][1] * sc)); }
      if (g < 2) {
        f32x4 w4 = g ? f32x4{a[4], a[5], a[6], a[7]} : f32x4{a[0], a[1], a[2], a[3]};
        *reinterpret_cast<f32x4*>(red + wave * 128 + 8 * c + 4 * g) = w4;
      }
      if (lane == 0) *reinterpret_cast<f32x2*>(wml + 2 * wave) = f32x2{mw, l};
    }
    wg_barrier();
    wg_barrier();
    {
      const u32x2 ov = *reinterpret_cast<const u32x2*>(os + (c >> 3) * 64 + 8 * wave + 2 * g);
      const bf16x8 a0 = sel_frag(ov[0] & 0xffffu, mask), a1 = sel_frag(ov[0] >> 16, mask);
      const bf16x8 a2 = sel_frag(ov[1] & 0xffffu, mask), a3 = sel_frag(ov[1] >> 16, mask);
      f32x4 y = {0.f, 0.f, 0.f, 0.f};
      y = __builtin_amdgcn_mfma_f32_16x16x32_bf16(a0, __builtin_bit_cast(bf16x8, wo[0]), y, 0, 0, 0);
      y = __builtin_amdgcn_mfma_f32_16x16x32_bf16(a1, __builtin_bit_cast(bf16x8, wo[1]), y, 0, 0, 0);
      y = __builtin_amdgcn_mfma_f32_16x16x32_bf16(a2, __builtin_bit_cast(bf16x8, wo[2]), y, 0, 0, 0);
      y = __builtin_amdgcn_mfma_f32_16x16x32_bf16(a3, __builtin_bit_cast(bf16x8, wo[3]), y, 0, 0, 0);
      *reinterpret_cast<f32x4*>(red + wave * 256 + (g >> 1) * 128 + 8 * c + 4 * (g & 1)) = y;
      if (mem == 0) {
        f32x4 z = {0.f, 0.f, 0.f, 0.f};
        z = __builtin_amdgcn_mfma_f32_16x16x32_bf16(a0, __builtin_bit_cast(bf16x8, wo[4]), z, 0, 0, 0);
        z = __builtin_amdgcn_mfma_f32_16x16x32_bf16(a1, __builtin_bit_cast(bf16x8, wo[5]), z, 0, 0, 0);
        z = __builtin_amdgcn_mfma_f32_16x16x32_bf16(a2, __builtin_bit_cast(bf16x8, wo[6]), z, 0, 0, 0);
        z = __builtin_amdgcn_mfma_f32_16x16x32_bf16(a3, __builtin_bit_cast(bf16x8, wo[7]), z, 0, 0, 0);
        *reinterpret_cast<f32x4*>(red + 2048 + wave * 256 + (g >> 1) * 128 + 8 * c + 4 * (g & 1)) = z;
      }
    }
    wg_barrier();
  } else {
    const unsigned epoch = __builtin_amdgcn_raw_buffer_load_b32(rws, WS_EPOCH + br * 256, 0, SC1);
    const unsigned tag = epoch + 1u;
    wg_barrier();
    {
      f32x4 s4 = *reinterpret_cast<const f32x4*>(red + 4 * lane);
#pragma unroll
      for (int w = 1; w < NW; ++w) s4 += *reinterpret_cast<const f32x4*>(red + w * 256 + 4 * lane);
      const int o = WS_QG + quad * 8192 + mem * 2048 + lane * 32;
      __builtin_amdgcn_raw_buffer_store_b128(u32x4{u(s4[0]), tag, u(s4[1]), tag}, rws, o, 0, SC1);
      __builtin_amdgcn_raw_buffer_store_b128(u32x4{u(s4[2]), tag, u(s4[3]), tag}, rws, o + 16, 0, SC1);
      const int go = WS_QG + quad * 8192 + b * 1024 + lane * 16;
      u32x4 r[4];
      for (unsigned spins = 0; spins < SPIN_CAP; ++spins) {
        bool ok = true;
#pragma unroll
        for (int mm = 0; mm < 4; ++mm) r[mm] = __builtin_amdgcn_raw_buffer_load_b128(rws, go + mm * 2048, 0, SC1);
#pragma unroll
        for (int mm = 0; mm < 4; ++mm) ok &= (r[mm][1] == tag) & (r[mm][3] == tag);
        if (__all(ok)) break;
        __builtin_amdgcn_s_sleep(1);
        asm volatile("" ::: "memory");
      }
      const float q0 = (f(r[0][0]) + f(r[1][0])) + (f(r[2][0]) + f(r[3][0]));
      const float q1 = (f(r[0][2]) + f(r[1][2])) + (f(r[2][2]) + f(r[3][2]));
      qs[lane] = pack_bf16(q0, q1);
    }
    wg_barrier();
    wg_barrier();
    {
      f32x4 ml[4];
#pragma unroll
      for (int i = 0; i < 4; ++i) ml[i] = *reinterpret_cast<const f32x4*>(wml + 4 * i);
      float M = -INFINITY;
#pragma unroll
      for (int i = 0; i < 4; ++i) M = fmaxf(M, fmaxf(ml[i][0], ml[i][2]));
      float L = 0.f; f32x2 o2 = {0.f, 0.f};
#pragma unroll
      for (int w = 0; w < NW; ++w) {
        const float sc = __builtin_amdgcn_exp2f((ml[w >> 1][(w & 1) * 2] - M) * C2);
        L = __builtin_fmaf(sc, ml[w >> 1][(w & 1) * 2 + 1], L);
        o2 += sc * *reinterpret_cast<const f32x2*>(red + w * 128 + 2 * lane);
      }
      const int po = WS_PG + (quad * 4 + mem) * 2048;
      __builtin_amdgcn_raw_buffer_store_b128(u32x4{u(o2[0]), tag, u(o2[1]), tag}, rws, po + lane * 16, 0, SC1);
      if (lane == 0) __builtin_amdgcn_raw_buffer_store_b128(u32x4{u(M), tag, u(L), tag}, rws, po + 1024, 0, SC1);
      const int go = WS_PG + quad * 4 * 2048;
      u32x4 r[4], t[4];
      for (unsigned spins = 0; spins < SPIN_CAP; ++spins) {
        bool ok = true;
#pragma unroll
        for (int mm = 0; mm < 4; ++mm) {
          r[mm] = __builtin_amdgcn_raw_buffer_load_b128(rws, go + mm * 2048 + lane * 16, 0, SC1);
          t[mm] = __builtin_amdgcn_raw_buffer_load_b128(rws, go + mm * 2048 + 1024, 0, SC1);
        }
#pragma unroll
        for (int mm = 0; mm < 4; ++mm) ok &= (r[mm][1] == tag) & (r[mm][3] == tag) & (t[mm][1] == tag) & (t[mm][3] == tag);
        if (__all(ok)) break;
        __builtin_amdgcn_s_sleep(1);
        asm volatile("" ::: "memory");
      }
#pragma unroll
      for (int bb = 0; bb < 2; ++bb) {
        const float m0 = f(t[2 * bb][0]), m1 = f(t[2 * bb + 1][0]);
        const float MM = fmaxf(m0, m1);
        const float w0 = __builtin_amdgcn_exp2f((m0 - MM) * C2), w1 = __builtin_amdgcn_exp2f((m1 - MM) * C2);
        const float inv = 1.0f / (w0 * f(t[2 * bb][2]) + w1 * f(t[2 * bb + 1][2]));
        const float a0 = (w0 * f(r[2 * bb][0]) + w1 * f(r[2 * bb + 1][0])) * inv;
        const float a1 = (w0 * f(r[2 * bb][2]) + w1 * f(r[2 * bb + 1][2])) * inv;
        os[bb * 64 + lane] = pack_bf16(a0, a1);
      }
    }
    wg_barrier();
    wg_barrier();
    {
      const int bb = lane >> 5, c4 = (lane & 31) * 4;
#pragma unroll
      for (int blk = 0; blk < 2; ++blk) {
        if (blk == 1 && mem != 0) break;
        const float* src = red + blk * 2048 + 4 * lane;
        f32x4 s4 = *reinterpret_cast<const f32x4*>(src);
#pragma unroll
        for (int w = 1; w < NW; ++w) s4 += *reinterpret_cast<const f32x4*>(src + w * 256);
        const int col = (blk ? 512 : mem * 128) + c4;
        const int o = WS_YG + (((br * 2 + bb) * 40 + (col >> 4)) * 32 + h) * 128 + (col & 15) * 8;
        __builtin_amdgcn_raw_buffer_store_b128(u32x4{u(s4[0]), tag, u(s4[1]), tag}, rws, o, 0, SC1);
        __builtin_amdgcn_raw_buffer_store_b128(u32x4{u(s4[2]), tag, u(s4[3]), tag}, rws, o + 16, 0, SC1);
      }
      const int wb = bid & 127;
      if (wb < 80) {
        const int go = WS_YG + (br * 80 + wb) * 4096 + lane * 16;
        u32x4 r[4];
        for (unsigned spins = 0; spins < SPIN_CAP; ++spins) {
          bool ok = true;
#pragma unroll
          for (int k = 0; k < 4; ++k) r[k] = __builtin_amdgcn_raw_buffer_load_b128(rws, go + k * 1024, 0, SC1);
#pragma unroll
          for (int k = 0; k < 4; ++k) ok &= (r[k][1] == tag) & (r[k][3] == tag);
          if (__all(ok)) break;
          __builtin_amdgcn_s_sleep(1);
          asm volatile("" ::: "memory");
        }
        float y0 = (f(r[0][0]) + f(r[1][0])) + (f(r[2][0]) + f(r[3][0]));
        float y1 = (f(r[0][2]) + f(r[1][2])) + (f(r[2][2]) + f(r[3][2]));
        y0 += dpp<0x128>(y0); y1 += dpp<0x128>(y1);
        y0 = x32_sum(x16_sum(y0)); y1 = x32_sum(x16_sum(y1));
        const int ob = wb / 40, cb = wb % 40;
        if (lane < 8) reinterpret_cast<unsigned*>(p.out)[(ob * 1280 + br * 640 + cb * 16) / 2 + lane] = pack_bf16(y0, y1);
        if (wb == 0 && lane == 0) __builtin_amdgcn_raw_buffer_store_b32(tag, rws, WS_EPOCH + br * 256, 0, SC1);
      }
    }
  }
}

extern "C" void kernel_launch(void* const* d_in, const int* in_sizes, int n_in, void* d_out, int out_size, void* d_ws,
                              size_t ws_size, hipStream_t stream) {
  Params p;
  p.x[0] = d_in[0]; p.k[0] = d_in[1]; p.v[0] = d_in[2]; p.wq[0] = d_in[3]; p.wo[0] = d_in[4];
  p.x[1] = d_in[5]; p.k[1] = d_in[6]; p.v[1] = d_in[7]; p.wq[1] = d_in[8]; p.wo[1] = d_in[9];
  p.out = d_out; p.ws = d_ws;
  constexpr int LDS_BYTES = 64 * 1024;
  fused_decode_kernel<<<256, NTHR, LDS_BYTES, stream>>>(p);
}
```

```cpp
#include <hip/hip_runtime.h>
#include <hip/hip_bf16.h>
#include <stdint.h>

typedef unsigned u32x4 __attribute__((ext_vector_type(4)));
typedef unsigned u32x2 __attribute__((ext_vector_type(2)));
typedef float f32x4 __attribute__((ext_vector_type(4)));
typedef float f32x2 __attribute__((ext_vector_type(2)));
typedef __attribute__((ext_vector_type(2))) __bf16 bf16x2;
typedef __attribute__((ext_vector_type(8))) __bf16 bf16x8;

#define SB() __builtin_amdgcn_sched_barrier(0)
#define KV_AUX 2
#define W_AUX 2
#define SC1 16

constexpr int NW = 8, H = 8, NTHR = (NW + 1) * 64;
constexpr int NIT = 512 / (NW * H);
constexpr unsigned SPIN_CAP = 1u << 18;

constexpr unsigned WS_EPOCH = 0;
constexpr unsigned WS_QG = 4096;
constexpr unsigned WS_PG = WS_QG + 64 * 8192;
constexpr unsigned WS_YG = WS_PG + 64 * 4 * 2048;
constexpr unsigned WS_END = WS_YG + 160 * 4096;

#define STAMP(i)
struct Params {
  const void* x[2]; const void* k[2]; const void* v[2]; const void* wq[2]; const void* wo[2];
  void* out; void* ws;
};

__device__ __forceinline__ float dot2(unsigned k, unsigned q, float acc) {
  return __builtin_amdgcn_fdot2_f32_bf16(__builtin_bit_cast(bf16x2, k), __builtin_bit_cast(bf16x2, q), acc, false);
}
template <int CTRL> __device__ __forceinline__ float dpp(float x) {
  return __builtin_bit_cast(float, __builtin_amdgcn_mov_dpp(__builtin_bit_cast(int, x), CTRL, 0xf, 0xf, true));
}
__device__ __forceinline__ float bflo(unsigned u) { return __builtin_bit_cast(float, u << 16); }
__device__ __forceinline__ float bfhi(unsigned u) { return __builtin_bit_cast(float, u & 0xffff0000u); }
__device__ __forceinline__ unsigned pack_bf16(float lo, float hi) {
  bf16x2 r; r[0] = (__bf16)lo; r[1] = (__bf16)hi;
  return __builtin_bit_cast(unsigned, r);
}
__device__ __forceinline__ float f(unsigned u) { return __builtin_bit_cast(float, u); }
__device__ __forceinline__ unsigned u(float x) { return __builtin_bit_cast(unsigned, x); }
__device__ __forceinline__ float x16_sum(float x) {
  auto s = __builtin_amdgcn_permlane16_swap(u(x), u(x), false, false);
  return f(s[0]) + f(s[1]);
}
__device__ __forceinline__ float x32_sum(float x) {
  auto s = __builtin_amdgcn_permlane32_swap(u(x), u(x), false, false);
  return f(s[0]) + f(s[1]);
}
__device__ __forceinline__ float x16_max(float x) {
  auto s = __builtin_amdgcn_permlane16_swap(u(x), u(x), false, false);
  return fmaxf(f(s[0]), f(s[1]));
}
__device__ __forceinline__ float x32_max(float x) {
  auto s = __builtin_amdgcn_permlane32_swap(u(x), u(x), false, false);
  return fmaxf(f(s[0]), f(s[1]));
}
__device__ __forceinline__ void wg_barrier() {
  asm volatile("s_waitcnt lgkmcnt(0)" ::: "memory");
  __builtin_amdgcn_s_barrier();
  asm volatile("" ::: "memory");
}
__device__ __forceinline__ bf16x8 sel_frag(unsigned val16, const u32x4& mask) {
  const unsigned pp = val16 * 0x10001u;
  u32x4 a = {pp & mask[0], pp & mask[1], pp & mask[2], pp & mask[3]};
  return __builtin_bit_cast(bf16x8, a);
}

#define MK_RSRC(ptr, bytes) __builtin_amdgcn_make_buffer_rsrc((void*)(ptr), 0, (bytes), 0x00020000)

__global__ void __launch_bounds__(NTHR, 1) fused_decode_kernel(Params p) {
  extern __shared__ __attribute__((aligned(16))) unsigned char smem[];
  float* red = reinterpret_cast<float*>(smem);
  float* wml = reinterpret_cast<float*>(smem + 16384);
  unsigned* qs = reinterpret_cast<unsigned*>(smem + 16384 + 64);
  unsigned* os = reinterpret_cast<unsigned*>(smem + 16384 + 64 + 256);

  const int bid = blockIdx.x;
  const int quad = bid >> 2, mem = bid & 3, br = quad >> 5, h = quad & 31, b = mem >> 1, half = mem & 1;
  const int tid = threadIdx.x, lane = tid & 63;
  const int wave = __builtin_amdgcn_readfirstlane(tid >> 6);
  const int g = lane >> 4, c = lane & 15;
  constexpr float C2 = 0.08838834764831845f * 1.4426950408889634f;
  auto rws = MK_RSRC(p.ws, WS_END);

  if (wave < NW) {
    const char* kb = (const char*)p.k[br] + ((size_t)(b * 32 + h) * 2 + half) * (512u << 10);
    const char* vb = (const char*)p.v[br] + ((size_t)(b * 32 + h) * 2 + half) * (512u << 10);
    auto rk = MK_RSRC(kb, 512 << 10);
    auto rv = MK_RSRC(vb, 512 << 10);
    auto rx = MK_RSRC(p.x[br], 2 * 640 * 2);
    auto rwq = MK_RSRC(p.wq[br], 640 * 4096 * 2);
    auto rwo = MK_RSRC(p.wo[br], 4096 * 640 * 2);
    u32x4 mask;
#pragma unroll
    for (int t = 0; t < 4; ++t) mask[t] = (((c & 7) >> 1) == t) ? ((c & 1) ? 0xffff0000u : 0x0000ffffu) : 0u;

    const int r0 = mem * 160;
    const int xoff = (c >> 3) * 1280 + (r0 + 16 * wave + 4 * g) * 2;
    u32x2 xa = __builtin_amdgcn_raw_buffer_load_b64(rx, xoff, 0, 0);
    unsigned xb = __builtin_amdgcn_raw_buffer_load_b16(rx, (c >> 3) * 1280 + (r0 + 128 + 4 * wave + g) * 2, 0, 0);
    SB();
    u32x4 wq[5];
    {
      const int wo_ = (r0 + 16 * wave + 4 * g) * 8192 + h * 256 + c * 16;
#pragma unroll
      for (int i = 0; i < 4; ++i) { wq[i] = __builtin_amdgcn_raw_buffer_load_b128(rwq, wo_ + i * 8192, 0, W_AUX); SB(); }
      wq[4] = __builtin_amdgcn_raw_buffer_load_b128(rwq, (r0 + 128 + 4 * wave + g) * 8192 + h * 256 + c * 16, 0, W_AUX);
      SB();
    }
    u32x4 kr[H], vr[H];
    const int lo = lane * 16;
    int so = wave * (H * 1024);
#pragma unroll
    for (int t = 0; t < H; ++t) { kr[t] = __builtin_amdgcn_raw_buffer_load_b128(rk, lo + t * 1024, so, KV_AUX); SB(); }
#pragma unroll
    for (int t = 0; t < H; ++t) { vr[t] = __builtin_amdgcn_raw_buffer_load_b128(rv, lo + t * 1024, so, KV_AUX); SB(); }
    {
      f32x4 qa = {0.f, 0.f, 0.f, 0.f};
      qa = __builtin_amdgcn_mfma_f32_16x16x32_bf16(sel_frag(xa[0] & 0xffffu, mask), __builtin_bit_cast(bf16x8, wq[0]), qa, 0, 0, 0);
      qa = __builtin_amdgcn_mfma_f32_16x16x32_bf16(sel_frag(xa[0] >> 16, mask), __builtin_bit_cast(bf16x8, wq[1]), qa, 0, 0, 0);
      qa = __builtin_amdgcn_mfma_f32_16x16x32_bf16(sel_frag(xa[1] & 0xffffu, mask), __builtin_bit_cast(bf16x8, wq[2]), qa, 0, 0, 0);
      qa = __builtin_amdgcn_mfma_f32_16x16x32_bf16(sel_frag(xa[1] >> 16, mask), __builtin_bit_cast(bf16x8, wq[3]), qa, 0, 0, 0);
      qa = __builtin_amdgcn_mfma_f32_16x16x32_bf16(sel_frag(xb & 0xffffu, mask), __builtin_bit_cast(bf16x8, wq[4]), qa, 0, 0, 0);
      *reinterpret_cast<f32x4*>(red + wave * 256 + (g >> 1) * 128 + 8 * c + 4 * (g & 1)) = qa;
    }
    wg_barrier();
    wg_barrier();
    const u32x4 q = *reinterpret_cast<const u32x4*>(qs + 4 * c);

    float m = -INFINITY, l = 0.f;
    f32x2 acc[4];
#pragma unroll
    for (int j = 0; j < 4; ++j) acc[j] = f32x2{0.f, 0.f};
    u32x4 wo[8];
    const int wo_off = (h * 128 + 16 * wave + 4 * g) * 1280 + c * 16;

    auto step = [&](auto last_tag, int nso) {
      constexpr bool LAST = decltype(last_tag)::value;
      float s[H];
#pragma unroll
      for (int t = 0; t < H; ++t) {
        float d0 = dot2(kr[t][0], q[0], 0.f), d1 = dot2(kr[t][1], q[1], 0.f);
        d0 = dot2(kr[t][2], q[2], d0); d1 = dot2(kr[t][3], q[3], d1);
        s[t] = d0 + d1;
        SB();
        if constexpr (!LAST) kr[t] = __builtin_amdgcn_raw_buffer_load_b128(rk, lo + t * 1024, nso, KV_AUX);
        else {
          if (t < 4) wo[t] = __builtin_amdgcn_raw_buffer_load_b128(rwo, wo_off + t * 1280, mem * 256, W_AUX);
          else if (mem == 0) wo[t] = __builtin_amdgcn_raw_buffer_load_b128(rwo, wo_off + (t - 4) * 1280, 1024, W_AUX);
        }
        SB();
      }
#pragma unroll
      for (int t = 0; t < H; ++t) s[t] += dpp<0x128>(s[t]);
#pragma unroll
      for (int t = 0; t < H; ++t) s[t] += dpp<0x124>(s[t]);
#pragma unroll
      for (int t = 0; t < H; ++t) s[t] += dpp<0x122>(s[t]);
#pragma unroll
      for (int t = 0; t < H; ++t) s[t] += dpp<0x121>(s[t]);
      float mn = m;
#pragma unroll
      for (int t = 0; t < H; ++t) mn = fmaxf(mn, s[t]);
      const float mc = mn * C2;
      const float alpha = __builtin_amdgcn_exp2f(m * C2 - mc);
      m = mn;
      float ps = 0.f;
#pragma unroll
      for (int t = 0; t < H; ++t) { s[t] = __builtin_amdgcn_exp2f(__builtin_fmaf(s[t], C2, -mc)); ps += s[t]; }
      l = __builtin_fmaf(l, alpha, ps);
#pragma unroll
      for (int j = 0; j < 4; ++j) acc[j] *= alpha;
      SB();
#pragma unroll
      for (int t = 0; t < H; ++t) {
        const f32x2 pp = {s[t], s[t]};
#pragma unroll
        for (int j = 0; j < 4; ++j) acc[j] = __builtin_elementwise_fma(f32x2{bflo(vr[t][j]), bfhi(vr[t][j])}, pp, acc[j]);
        SB();
        if constexpr (!LAST) vr[t] = __builtin_amdgcn_raw_buffer_load_b128(rv, lo + t * 1024, nso, KV_AUX);
        SB();
      }
    };
    for (int it = 1; it < NIT; ++it) {
      so += NW * H * 1024;
      step(std::false_type{}, so);
    }
    step(std::true_type{}, 0);

    if (wave == 0) STAMP(9);
    {
      const float mw = x32_max(x16_max(m));
      const float sc = __builtin_amdgcn_exp2f((m - mw) * C2);
      l = x32_sum(x16_sum(l * sc));
      float a[8];
#pragma unroll
      for (int j = 0; j < 4; ++j) { a[2 * j] = x32_sum(x16_sum(acc[j][0] * sc)); a[2 * j + 1] = x32_sum(x16_sum(acc[j][1] * sc)); }
      if (g < 2) {
        f32x4 w4 = g ? f32x4{a[4], a[5], a[6], a[7]} : f32x4{a[0], a[1], a[2], a[3]};
        *reinterpret_cast<f32x4*>(red + wave * 128 + 8 * c + 4 * g) = w4;
      }
      if (lane == 0) *reinterpret_cast<f32x2*>(wml + 2 * wave) = f32x2{mw, l};
    }
    wg_barrier();
    wg_barrier();
    {
      const u32x2 ov = *reinterpret_cast<const u32x2*>(os + (c >> 3) * 64 + 8 * wave + 2 * g);
      const bf16x8 a0 = sel_frag(ov[0] & 0xffffu, mask), a1 = sel_frag(ov[0] >> 16, mask);
      const bf16x8 a2 = sel_frag(ov[1] & 0xffffu, mask), a3 = sel_frag(ov[1] >> 16, mask);
      f32x4 y = {0.f, 0.f, 0.f, 0.f};
      y = __builtin_amdgcn_mfma_f32_16x16x32_bf16(a0, __builtin_bit_cast(bf16x8, wo[0]), y, 0, 0, 0);
      y = __builtin_amdgcn_mfma_f32_16x16x32_bf16(a1, __builtin_bit_cast(bf16x8, wo[1]), y, 0, 0, 0);
      y = __builtin_amdgcn_mfma_f32_16x16x32_bf16(a2, __builtin_bit_cast(bf16x8, wo[2]), y, 0, 0, 0);
      y = __builtin_amdgcn_mfma_f32_16x16x32_bf16(a3, __builtin_bit_cast(bf16x8, wo[3]), y, 0, 0, 0);
      *reinterpret_cast<f32x4*>(red + wave * 256 + (g >> 1) * 128 + 8 * c + 4 * (g & 1)) = y;
      if (mem == 0) {
        f32x4 z = {0.f, 0.f, 0.f, 0.f};
        z = __builtin_amdgcn_mfma_f32_16x16x32_bf16(a0, __builtin_bit_cast(bf16x8, wo[4]), z, 0, 0, 0);
        z = __builtin_amdgcn_mfma_f32_16x16x32_bf16(a1, __builtin_bit_cast(bf16x8, wo[5]), z, 0, 0, 0);
        z = __builtin_amdgcn_mfma_f32_16x16x32_bf16(a2, __builtin_bit_cast(bf16x8, wo[6]), z, 0, 0, 0);
        z = __builtin_amdgcn_mfma_f32_16x16x32_bf16(a3, __builtin_bit_cast(bf16x8, wo[7]), z, 0, 0, 0);
        *reinterpret_cast<f32x4*>(red + 2048 + wave * 256 + (g >> 1) * 128 + 8 * c + 4 * (g & 1)) = z;
      }
    }
    wg_barrier();
  } else {
    STAMP(0); STAMP(8);
    const unsigned epoch = __builtin_amdgcn_raw_buffer_load_b32(rws, WS_EPOCH + br * 256, 0, SC1);
    const unsigned tag = epoch + 1u;
    wg_barrier();
    STAMP(1);
    {
      f32x4 s4 = *reinterpret_cast<const f32x4*>(red + 4 * lane);
#pragma unroll
      for (int w = 1; w < NW; ++w) s4 += *reinterpret_cast<const f32x4*>(red + w * 256 + 4 * lane);
      const int o = WS_QG + quad * 8192 + mem * 2048 + lane * 32;
      __builtin_amdgcn_raw_buffer_store_b128(u32x4{u(s4[0]), tag, u(s4[1]), tag}, rws, o, 0, SC1);
      __builtin_amdgcn_raw_buffer_store_b128(u32x4{u(s4[2]), tag, u(s4[3]), tag}, rws, o + 16, 0, SC1);
      const int go = WS_QG + quad * 8192 + b * 1024 + lane * 16;
      u32x4 r[4];
      for (unsigned spins = 0; spins < SPIN_CAP; ++spins) {
        bool ok = true;
#pragma unroll
        for (int mm = 0; mm < 4; ++mm) r[mm] = __builtin_amdgcn_raw_buffer_load_b128(rws, go + mm * 2048, 0, SC1);
#pragma unroll
        for (int mm = 0; mm < 4; ++mm) ok &= (r[mm][1] == tag) & (r[mm][3] == tag);
        if (__all(ok)) break;
        __builtin_amdgcn_s_sleep(1);
        asm volatile("" ::: "memory");
      }
      const float q0 = (f(r[0][0]) + f(r[1][0])) + (f(r[2][0]) + f(r[3][0]));
      const float q1 = (f(r[0][2]) + f(r[1][2])) + (f(r[2][2]) + f(r[3][2]));
      qs[lane] = pack_bf16(q0, q1);
    }
    STAMP(2);
    wg_barrier();
    wg_barrier();
    STAMP(3);
    {
      f32x4 ml[4];
#pragma unroll
      for (int i = 0; i < 4; ++i) ml[i] = *reinterpret_cast<const f32x4*>(wml + 4 * i);
      float M = -INFINITY;
#pragma unroll
      for (int i = 0; i < 4; ++i) M = fmaxf(M, fmaxf(ml[i][0], ml[i][2]));
      float L = 0.f; f32x2 o2 = {0.f, 0.f};
#pragma unroll
      for (int w = 0; w < NW; ++w) {
        const float sc = __builtin_amdgcn_exp2f((ml[w >> 1][(w & 1) * 2] - M) * C2);
        L = __builtin_fmaf(sc, ml[w >> 1][(w & 1) * 2 + 1], L);
        o2 += sc * *reinterpret_cast<const f32x2*>(red + w * 128 + 2 * lane);
      }
      const int po = WS_PG + (quad * 4 + mem) * 2048;
      __builtin_amdgcn_raw_buffer_store_b128(u32x4{u(o2[0]), tag, u(o2[1]), tag}, rws, po + lane * 16, 0, SC1);
      if (lane == 0) __builtin_amdgcn_raw_buffer_store_b128(u32x4{u(M), tag, u(L), tag}, rws, po + 1024, 0, SC1);
      STAMP(4);
      const int go = WS_PG + quad * 4 * 2048;
      u32x4 r[4], t[4];
      for (unsigned spins = 0; spins < SPIN_CAP; ++spins) {
        bool ok = true;
#pragma unroll
        for (int mm = 0; mm < 4; ++mm) {
          r[mm] = __builtin_amdgcn_raw_buffer_load_b128(rws, go + mm * 2048 + lane * 16, 0, SC1);
          t[mm] = __builtin_amdgcn_raw_buffer_load_b128(rws, go + mm * 2048 + 1024, 0, SC1);
        }
#pragma unroll
        for (int mm = 0; mm < 4; ++mm) ok &= (r[mm][1] == tag) & (r[mm][3] == tag) & (t[mm][1] == tag) & (t[mm][3] == tag);
        if (__all(ok)) break;
        __builtin_amdgcn_s_sleep(1);
        asm volatile("" ::: "memory");
      }
#pragma unroll
      for (int bb = 0; bb < 2; ++bb) {
        const float m0 = f(t[2 * bb][0]), m1 = f(t[2 * bb + 1][0]);
        const float MM = fmaxf(m0, m1);
        const float w0 = __builtin_amdgcn_exp2f((m0 - MM) * C2), w1 = __builtin_amdgcn_exp2f((m1 - MM) * C2);
        const float inv = 1.0f / (w0 * f(t[2 * bb][2]) + w1 * f(t[2 * bb + 1][2]));
        const float a0 = (w0 * f(r[2 * bb][0]) + w1 * f(r[2 * bb + 1][0])) * inv;
        const float a1 = (w0 * f(r[2 * bb][2]) + w1 * f(r[2 * bb + 1][2])) * inv;
        os[bb * 64 + lane] = pack_bf16(a0, a1);
      }
    }
    STAMP(5);
    wg_barrier();
    wg_barrier();
    STAMP(6);
    {
      const int bb = lane >> 5, c4 = (lane & 31) * 4;
#pragma unroll
      for (int blk = 0; blk < 2; ++blk) {
        if (blk == 1 && mem != 0) break;
        const float* src = red + blk * 2048 + 4 * lane;
        f32x4 s4 = *reinterpret_cast<const f32x4*>(src);
#pragma unroll
        for (int w = 1; w < NW; ++w) s4 += *reinterpret_cast<const f32x4*>(src + w * 256);
        const int col = (blk ? 512 : mem * 128) + c4;
        const int o = WS_YG + (((br * 2 + bb) * 40 + (col >> 4)) * 32 + h) * 128 + (col & 15) * 8;
        __builtin_amdgcn_raw_buffer_store_b128(u32x4{u(s4[0]), tag, u(s4[1]), tag}, rws, o, 0, SC1);
        __builtin_amdgcn_raw_buffer_store_b128(u32x4{u(s4[2]), tag, u(s4[3]), tag}, rws, o + 16, 0, SC1);
      }
      STAMP(7);
      const int wb = bid & 127;
      if (wb < 80) {
        const int go = WS_YG + (br * 80 + wb) * 4096 + lane * 16;
        u32x4 r[4];
        for (unsigned spins = 0; spins < SPIN_CAP; ++spins) {
          bool ok = true;
#pragma unroll
          for (int k = 0; k < 4; ++k) r[k] = __builtin_amdgcn_raw_buffer_load_b128(rws, go + k * 1024, 0, SC1);
#pragma unroll
          for (int k = 0; k < 4; ++k) ok &= (r[k][1] == tag) & (r[k][3] == tag);
          if (__all(ok)) break;
          __builtin_amdgcn_s_sleep(1);
          asm volatile("" ::: "memory");
        }
        float y0 = (f(r[0][0]) + f(r[1][0])) + (f(r[2][0]) + f(r[3][0]));
        float y1 = (f(r[0][2]) + f(r[1][2])) + (f(r[2][2]) + f(r[3][2]));
        y0 += dpp<0x128>(y0); y1 += dpp<0x128>(y1);
        y0 = x32_sum(x16_sum(y0)); y1 = x32_sum(x16_sum(y1));
        const int ob = wb / 40, cb = wb % 40;
        if (lane < 8) reinterpret_cast<unsigned*>(p.out)[(ob * 1280 + br * 640 + cb * 16) / 2 + lane] = pack_bf16(y0, y1);
        STAMP(8);
        if (wb == 0 && lane == 0) __builtin_amdgcn_raw_buffer_store_b32(tag, rws, WS_EPOCH + br * 256, 0, SC1);
      }
    }
  }
}

extern "C" void kernel_launch(void* const* d_in, const int* in_sizes, int n_in, void* d_out, int out_size, void* d_ws,
                              size_t ws_size, hipStream_t stream) {
  Params p;
  p.x[0] = d_in[0]; p.k[0] = d_in[1]; p.v[0] = d_in[2]; p.wq[0] = d_in[3]; p.wo[0] = d_in[4];
  p.x[1] = d_in[5]; p.k[1] = d_in[6]; p.v[1] = d_in[7]; p.wq[1] = d_in[8]; p.wo[1] = d_in[9];
  p.out = d_out; p.ws = d_ws;
  constexpr int LDS_BYTES = 64 * 1024;
  fused_decode_kernel<<<256, NTHR, LDS_BYTES, stream>>>(p);
}
```

```cpp
#define AUX_A 0
#define AUX_B 2
#include <hip/hip_runtime.h>
#include <hip/hip_bf16.h>
#include <stdint.h>

typedef unsigned u32x4 __attribute__((ext_vector_type(4)));
typedef unsigned u32x2 __attribute__((ext_vector_type(2)));
typedef float f32x4 __attribute__((ext_vector_type(4)));
typedef float f32x2 __attribute__((ext_vector_type(2)));
typedef __attribute__((ext_vector_type(2))) __bf16 bf16x2;
typedef __attribute__((ext_vector_type(8))) __bf16 bf16x8;

#define SB() __builtin_amdgcn_sched_barrier(0)
#define W_AUX 2
#define SC1 16

constexpr int NW = 8, H = 8, NTHR = (NW + 1) * 64;
constexpr int NIT = 512 / (NW * H);
constexpr unsigned SPIN_CAP = 1u << 18;

constexpr unsigned WS_EPOCH = 0;
constexpr unsigned WS_QG = 4096;
constexpr unsigned WS_PG = WS_QG + 64 * 8192;
constexpr unsigned WS_YG = WS_PG + 64 * 4 * 2048;
constexpr unsigned WS_END = WS_YG + 160 * 4096;

#define STAMP(i)
struct Params {
  const void* x[2]; const void* k[2]; const void* v[2]; const void* wq[2]; const void* wo[2];
  void* out; void* ws;
};

__device__ __forceinline__ float dot2(unsigned k, unsigned q, float acc) {
  return __builtin_amdgcn_fdot2_f32_bf16(__builtin_bit_cast(bf16x2, k), __builtin_bit_cast(bf16x2, q), acc, false);
}
template <int CTRL> __device__ __forceinline__ float dpp(float x) {
  return __builtin_bit_cast(float, __builtin_amdgcn_mov_dpp(__builtin_bit_cast(int, x), CTRL, 0xf, 0xf, true));
}
__device__ __forceinline__ float bflo(unsigned u) { return __builtin_bit_cast(float, u << 16); }
__device__ __forceinline__ float bfhi(unsigned u) { return __builtin_bit_cast(float, u & 0xffff0000u); }
__device__ __forceinline__ unsigned pack_bf16(float lo, float hi) {
  bf16x2 r; r[0] = (__bf16)lo; r[1] = (__bf16)hi;
  return __builtin_bit_cast(unsigned, r);
}
__device__ __forceinline__ float f(unsigned u) { return __builtin_bit_cast(float, u); }
__device__ __forceinline__ unsigned u(float x) { return __builtin_bit_cast(unsigned, x); }
__device__ __forceinline__ float x16_sum(float x) {
  auto s = __builtin_amdgcn_permlane16_swap(u(x), u(x), false, false);
  return f(s[0]) + f(s[1]);
}
__device__ __forceinline__ float x32_sum(float x) {
  auto s = __builtin_amdgcn_permlane32_swap(u(x), u(x), false, false);
  return f(s[0]) + f(s[1]);
}
__device__ __forceinline__ float x16_max(float x) {
  auto s = __builtin_amdgcn_permlane16_swap(u(x), u(x), false, false);
  return fmaxf(f(s[0]), f(s[1]));
}
__device__ __forceinline__ float x32_max(float x) {
  auto s = __builtin_amdgcn_permlane32_swap(u(x), u(x), false, false);
  return fmaxf(f(s[0]), f(s[1]));
}
__device__ __forceinline__ void wg_barrier() {
  asm volatile("s_waitcnt lgkmcnt(0)" ::: "memory");
  __builtin_amdgcn_s_barrier();
  asm volatile("" ::: "memory");
}
__device__ __forceinline__ bf16x8 sel_frag(unsigned val16, const u32x4& mask) {
  const unsigned pp = val16 * 0x10001u;
  u32x4 a = {pp & mask[0], pp & mask[1], pp & mask[2], pp & mask[3]};
  return __builtin_bit_cast(bf16x8, a);
}

#define MK_RSRC(ptr, bytes) __builtin_amdgcn_make_buffer_rsrc((void*)(ptr), 0, (bytes), 0x00020000)

template <int KV_AUX>
__device__ __forceinline__ void body(const Params& p) {
  extern __shared__ __attribute__((aligned(16))) unsigned char smem[];
  float* red = reinterpret_cast<float*>(smem);
  float* wml = reinterpret_cast<float*>(smem + 16384);
  unsigned* qs = reinterpret_cast<unsigned*>(smem + 16384 + 64);
  unsigned* os = reinterpret_cast<unsigned*>(smem + 16384 + 64 + 256);

  const int bid = blockIdx.x;
  const int quad = bid >> 2, mem = bid & 3, br = quad >> 5, h = quad & 31, b = mem >> 1, half = mem & 1;
  const int tid = threadIdx.x, lane = tid & 63;
  const int wave = __builtin_amdgcn_readfirstlane(tid >> 6);
  const int g = lane >> 4, c = lane & 15;
  constexpr float C2 = 0.08838834764831845f * 1.4426950408889634f;
  auto rws = MK_RSRC(p.ws, WS_END);

  if (wave < NW) {
    const char* kb = (const char*)p.k[br] + ((size_t)(b * 32 + h) * 2 + half) * (512u << 10);
    const char* vb = (const char*)p.v[br] + ((size_t)(b * 32 + h) * 2 + half) * (512u << 10);
    auto rk = MK_RSRC(kb, 512 << 10);
    auto rv = MK_RSRC(vb, 512 << 10);
    auto rx = MK_RSRC(p.x[br], 2 * 640 * 2);
    auto rwq = MK_RSRC(p.wq[br], 640 * 4096 * 2);
    auto rwo = MK_RSRC(p.wo[br], 4096 * 640 * 2);
    u32x4 mask;
#pragma unroll
    for (int t = 0; t < 4; ++t) mask[t] = (((c & 7) >> 1) == t) ? ((c & 1) ? 0xffff0000u : 0x0000ffffu) : 0u;

    const int r0 = mem * 160;
    const int xoff = (c >> 3) * 1280 + (r0 + 16 * wave + 4 * g) * 2;
    u32x2 xa = __builtin_amdgcn_raw_buffer_load_b64(rx, xoff, 0, 0);
    unsigned xb = __builtin_amdgcn_raw_buffer_load_b16(rx, (c >> 3) * 1280 + (r0 + 128 + 4 * wave + g) * 2, 0, 0);
    SB();
    u32x4 wq[5];
    {
      const int wo_ = (r0 + 16 * wave + 4 * g) * 8192 + h * 256 + c * 16;
#pragma unroll
      for (int i = 0; i < 4; ++i) { wq[i] = __builtin_amdgcn_raw_buffer_load_b128(rwq, wo_ + i * 8192, 0, W_AUX); SB(); }
      wq[4] = __builtin_amdgcn_raw_buffer_load_b128(rwq, (r0 + 128 + 4 * wave + g) * 8192 + h * 256 + c * 16, 0, W_AUX);
      SB();
    }
    u32x4 kr[H], vr[H];
    const int lo = lane * 16;
    int so = wave * (H * 1024);
#pragma unroll
    for (int t = 0; t < H; ++t) { kr[t] = __builtin_amdgcn_raw_buffer_load_b128(rk, lo + t * 1024, so, KV_AUX); SB(); }
#pragma unroll
    for (int t = 0; t < H; ++t) { vr[t] = __builtin_amdgcn_raw_buffer_load_b128(rv, lo + t * 1024, so, KV_AUX); SB(); }
    {
      f32x4 qa = {0.f, 0.f, 0.f, 0.f};
      qa = __builtin_amdgcn_mfma_f32_16x16x32_bf16(sel_frag(xa[0] & 0xffffu, mask), __builtin_bit_cast(bf16x8, wq[0]), qa, 0, 0, 0);
      qa = __builtin_amdgcn_mfma_f32_16x16x32_bf16(sel_frag(xa[0] >> 16, mask), __builtin_bit_cast(bf16x8, wq[1]), qa, 0, 0, 0);
      qa = __builtin_amdgcn_mfma_f32_16x16x32_bf16(sel_frag(xa[1] & 0xffffu, mask), __builtin_bit_cast(bf16x8, wq[2]), qa, 0, 0, 0);
      qa = __builtin_amdgcn_mfma_f32_16x16x32_bf16(sel_frag(xa[1] >> 16, mask), __builtin_bit_cast(bf16x8, wq[3]), qa, 0, 0, 0);
      qa = __builtin_amdgcn_mfma_f32_16x16x32_bf16(sel_frag(xb & 0xffffu, mask), __builtin_bit_cast(bf16x8, wq[4]), qa, 0, 0, 0);
      *reinterpret_cast<f32x4*>(red + wave * 256 + (g >> 1) * 128 + 8 * c + 4 * (g & 1)) = qa;
    }
    wg_barrier();
    wg_barrier();
    const u32x4 q = *reinterpret_cast<const u32x4*>(qs + 4 * c);

    float m = -INFINITY, l = 0.f;
    f32x2 acc[4];
#pragma unroll
    for (int j = 0; j < 4; ++j) acc[j] = f32x2{0.f, 0.f};
    u32x4 wo[8];
    const int wo_off = (h * 128 + 16 * wave + 4 * g) * 1280 + c * 16;

    auto step = [&](auto last_tag, int nso) {
      constexpr bool LAST = decltype(last_tag)::value;
      float s[H];
#pragma unroll
      for (int t = 0; t < H; ++t) {
        float d0 = dot2(kr[t][0], q[0], 0.f), d1 = dot2(kr[t][1], q[1], 0.f);
        d0 = dot2(kr[t][2], q[2], d0); d1 = dot2(kr[t][3], q[3], d1);
        s[t] = d0 + d1;
        SB();
        if constexpr (!LAST) kr[t] = __builtin_amdgcn_raw_buffer_load_b128(rk, lo + t * 1024, nso, KV_AUX);
        else {
          if (t < 4) wo[t] = __builtin_amdgcn_raw_buffer_load_b128(rwo, wo_off + t * 1280, mem * 256, W_AUX);
          else if (mem == 0) wo[t] = __builtin_amdgcn_raw_buffer_load_b128(rwo, wo_off + (t - 4) * 1280, 1024, W_AUX);
        }
        SB();
      }
#pragma unroll
      for (int t = 0; t < H; ++t) s[t] += dpp<0x128>(s[t]);
#pragma unroll
      for (int t = 0; t < H; ++t) s[t] += dpp<0x124>(s[t]);
#pragma unroll
      for (int t = 0; t < H; ++t) s[t] += dpp<0x122>(s[t]);
#pragma unroll
      for (int t = 0; t < H; ++t) s[t] += dpp<0x121>(s[t]);
      float mn = m;
#pragma unroll
      for (int t = 0; t < H; ++t) mn = fmaxf(mn, s[t]);
      const float mc = mn * C2;
      const float alpha = __builtin_amdgcn_exp2f(m * C2 - mc);
      m = mn;
      float ps = 0.f;
#pragma unroll
      for (int t = 0; t < H; ++t) { s[t] = __builtin_amdgcn_exp2f(__builtin_fmaf(s[t], C2, -mc)); ps += s[t]; }
      l = __builtin_fmaf(l, alpha, ps);
#pragma unroll
      for (int j = 0; j < 4; ++j) acc[j] *= alpha;
      SB();
#pragma unroll
      for (int t = 0; t < H; ++t) {
        const f32x2 pp = {s[t], s[t]};
#pragma unroll
        for (int j = 0; j < 4; ++j) acc[j] = __builtin_elementwise_fma(f32x2{bflo(vr[t][j]), bfhi(vr[t][j])}, pp, acc[j]);
        SB();
        if constexpr (!LAST) vr[t] = __builtin_amdgcn_raw_buffer_load_b128(rv, lo + t * 1024, nso, KV_AUX);
        SB();
      }
    };
    for (int it = 1; it < NIT; ++it) {
      so += NW * H * 1024;
      step(std::false_type{}, so);
    }
    step(std::true_type{}, 0);

    if (wave == 0) STAMP(9);
    {
      const float mw = x32_max(x16_max(m));
      const float sc = __builtin_amdgcn_exp2f((m - mw) * C2);
      l = x32_sum(x16_sum(l * sc));
      float a[8];
#pragma unroll
      for (int j = 0; j < 4; ++j) { a[2 * j] = x32_sum(x16_sum(acc[j][0] * sc)); a[2 * j + 1] = x32_sum(x16_sum(acc[j][1] * sc)); }
      if (g < 2) {
        f32x4 w4 = g ? f32x4{a[4], a[5], a[6], a[7]} : f32x4{a[0], a[1], a[2], a[3]};
        *reinterpret_cast<f32x4*>(red + wave * 128 + 8 * c + 4 * g) = w4;
      }
      if (lane == 0) *reinterpret_cast<f32x2*>(wml + 2 * wave) = f32x2{mw, l};
    }
    wg_barrier();
    wg_barrier();
    {
      const u32x2 ov = *reinterpret_cast<const u32x2*>(os + (c >> 3) * 64 + 8 * wave + 2 * g);
      const bf16x8 a0 = sel_frag(ov[0] & 0xffffu, mask), a1 = sel_frag(ov[0] >> 16, mask);
      const bf16x8 a2 = sel_frag(ov[1] & 0xffffu, mask), a3 = sel_frag(ov[1] >> 16, mask);
      f32x4 y = {0.f, 0.f, 0.f, 0.f};
      y = __builtin_amdgcn_mfma_f32_16x16x32_bf16(a0, __builtin_bit_cast(bf16x8, wo[0]), y, 0, 0, 0);
      y = __builtin_amdgcn_mfma_f32_16x16x32_bf16(a1, __builtin_bit_cast(bf16x8, wo[1]), y, 0, 0, 0);
      y = __builtin_amdgcn_mfma_f32_16x16x32_bf16(a2, __builtin_bit_cast(bf16x8, wo[2]), y, 0, 0, 0);
      y = __builtin_amdgcn_mfma_f32_16x16x32_bf16(a3, __builtin_bit_cast(bf16x8, wo[3]), y, 0, 0, 0);
      *reinterpret_cast<f32x4*>(red + wave * 256 + (g >> 1) * 128 + 8 * c + 4 * (g & 1)) = y;
      if (mem == 0) {
        f32x4 z = {0.f, 0.f, 0.f, 0.f};
        z = __builtin_amdgcn_mfma_f32_16x16x32_bf16(a0, __builtin_bit_cast(bf16x8, wo[4]), z, 0, 0, 0);
        z = __builtin_amdgcn_mfma_f32_16x16x32_bf16(a1, __builtin_bit_cast(bf16x8, wo[5]), z, 0, 0, 0);
        z = __builtin_amdgcn_mfma_f32_16x16x32_bf16(a2, __builtin_bit_cast(bf16x8, wo[6]), z, 0, 0, 0);
        z = __builtin_amdgcn_mfma_f32_16x16x32_bf16(a3, __builtin_bit_cast(bf16x8, wo[7]), z, 0, 0, 0);
        *reinterpret_cast<f32x4*>(red + 2048 + wave * 256 + (g >> 1) * 128 + 8 * c + 4 * (g & 1)) = z;
      }
    }
    wg_barrier();
  } else {
    STAMP(0); STAMP(8);
    const unsigned epoch = __builtin_amdgcn_raw_buffer_load_b32(rws, WS_EPOCH + br * 256, 0, SC1);
    const unsigned tag = epoch + 1u;
    wg_barrier();
    STAMP(1);
    {
      f32x4 s4 = *reinterpret_cast<const f32x4*>(red + 4 * lane);
#pragma unroll
      for (int w = 1; w < NW; ++w) s4 += *reinterpret_cast<const f32x4*>(red + w * 256 + 4 * lane);
      const int o = WS_QG + quad * 8192 + mem * 2048 + lane * 32;
      __builtin_amdgcn_raw_buffer_store_b128(u32x4{u(s4[0]), tag, u(s4[1]), tag}, rws, o, 0, SC1);
      __builtin_amdgcn_raw_buffer_store_b128(u32x4{u(s4[2]), tag, u(s4[3]), tag}, rws, o + 16, 0, SC1);
      const int go = WS_QG + quad * 8192 + b * 1024 + lane * 16;
      u32x4 r[4];
      for (unsigned spins = 0; spins < SPIN_CAP; ++spins) {
        bool ok = true;
#pragma unroll
        for (int mm = 0; mm < 4; ++mm) r[mm] = __builtin_amdgcn_raw_buffer_load_b128(rws, go + mm * 2048, 0, SC1);
#pragma unroll
        for (int mm = 0; mm < 4; ++mm) ok &= (r[mm][1] == tag) & (r[mm][3] == tag);
        if (__all(ok)) break;
        __builtin_amdgcn_s_sleep(1);
        asm volatile("" ::: "memory");
      }
      const float q0 = (f(r[0][0]) + f(r[1][0])) + (f(r[2][0]) + f(r[3][0]));
      const float q1 = (f(r[0][2]) + f(r[1][2])) + (f(r[2][2]) + f(r[3][2]));
      qs[lane] = pack_bf16(q0, q1);
    }
    STAMP(2);
    wg_barrier();
    wg_barrier();
    STAMP(3);
    {
      f32x4 ml[4];
#pragma unroll
      for (int i = 0; i < 4; ++i) ml[i] = *reinterpret_cast<const f32x4*>(wml + 4 * i);
      float M = -INFINITY;
#pragma unroll
      for (int i = 0; i < 4; ++i) M = fmaxf(M, fmaxf(ml[i][0], ml[i][2]));
      float L = 0.f; f32x2 o2 = {0.f, 0.f};
#pragma unroll
      for (int w = 0; w < NW; ++w) {
        const float sc = __builtin_amdgcn_exp2f((ml[w >> 1][(w & 1) * 2] - M) * C2);
        L = __builtin_fmaf(sc, ml[w >> 1][(w & 1) * 2 + 1], L);
        o2 += sc * *reinterpret_cast<const f32x2*>(red + w * 128 + 2 * lane);
      }
      const int po = WS_PG + (quad * 4 + mem) * 2048;
      __builtin_amdgcn_raw_buffer_store_b128(u32x4{u(o2[0]), tag, u(o2[1]), tag}, rws, po + lane * 16, 0, SC1);
      if (lane == 0) __builtin_amdgcn_raw_buffer_store_b128(u32x4{u(M), tag, u(L), tag}, rws, po + 1024, 0, SC1);
      STAMP(4);
      const int go = WS_PG + quad * 4 * 2048;
      u32x4 r[4], t[4];
      for (unsigned spins = 0; spins < SPIN_CAP; ++spins) {
        bool ok = true;
#pragma unroll
        for (int mm = 0; mm < 4; ++mm) {
          r[mm] = __builtin_amdgcn_raw_buffer_load_b128(rws, go + mm * 2048 + lane * 16, 0, SC1);
          t[mm] = __builtin_amdgcn_raw_buffer_load_b128(rws, go + mm * 2048 + 1024, 0, SC1);
        }
#pragma unroll
        for (int mm = 0; mm < 4; ++mm) ok &= (r[mm][1] == tag) & (r[mm][3] == tag) & (t[mm][1] == tag) & (t[mm][3] == tag);
        if (__all(ok)) break;
        __builtin_amdgcn_s_sleep(1);
        asm volatile("" ::: "memory");
      }
#pragma unroll
      for (int bb = 0; bb < 2; ++bb) {
        const float m0 = f(t[2 * bb][0]), m1 = f(t[2 * bb + 1][0]);
        const float MM = fmaxf(m0, m1);
        const float w0 = __builtin_amdgcn_exp2f((m0 - MM) * C2), w1 = __builtin_amdgcn_exp2f((m1 - MM) * C2);
        const float inv = 1.0f / (w0 * f(t[2 * bb][2]) + w1 * f(t[2 * bb + 1][2]));
        const float a0 = (w0 * f(r[2 * bb][0]) + w1 * f(r[2 * bb + 1][0])) * inv;
        const float a1 = (w0 * f(r[2 * bb][2]) + w1 * f(r[2 * bb + 1][2])) * inv;
        os[bb * 64 + lane] = pack_bf16(a0, a1);
      }
    }
    STAMP(5);
    wg_barrier();
    wg_barrier();
    STAMP(6);
    {
      const int bb = lane >> 5, c4 = (lane & 31) * 4;
#pragma unroll
      for (int blk = 0; blk < 2; ++blk) {
        if (blk == 1 && mem != 0) break;
        const float* src = red + blk * 2048 + 4 * lane;
        f32x4 s4 = *reinterpret_cast<const f32x4*>(src);
#pragma unroll
        for (int w = 1; w < NW; ++w) s4 += *reinterpret_cast<const f32x4*>(src + w * 256);
        const int col = (blk ? 512 : mem * 128) + c4;
        const int o = WS_YG + (((br * 2 + bb) * 40 + (col >> 4)) * 32 + h) * 128 + (col & 15) * 8;
        __builtin_amdgcn_raw_buffer_store_b128(u32x4{u(s4[0]), tag, u(s4[1]), tag}, rws, o, 0, SC1);
        __builtin_amdgcn_raw_buffer_store_b128(u32x4{u(s4[2]), tag, u(s4[3]), tag}, rws, o + 16, 0, SC1);
      }
      STAMP(7);
      const int wb = bid & 127;
      if (wb < 80) {
        const int go = WS_YG + (br * 80 + wb) * 4096 + lane * 16;
        u32x4 r[4];
        for (unsigned spins = 0; spins < SPIN_CAP; ++spins) {
          bool ok = true;
#pragma unroll
          for (int k = 0; k < 4; ++k) r[k] = __builtin_amdgcn_raw_buffer_load_b128(rws, go + k * 1024, 0, SC1);
#pragma unroll
          for (int k = 0; k < 4; ++k) ok &= (r[k][1] == tag) & (r[k][3] == tag);
          if (__all(ok)) break;
          __builtin_amdgcn_s_sleep(1);
          asm volatile("" ::: "memory");
        }
        float y0 = (f(r[0][0]) + f(r[1][0])) + (f(r[2][0]) + f(r[3][0]));
        float y1 = (f(r[0][2]) + f(r[1][2])) + (f(r[2][2]) + f(r[3][2]));
        y0 += dpp<0x128>(y0); y1 += dpp<0x128>(y1);
        y0 = x32_sum(x16_sum(y0)); y1 = x32_sum(x16_sum(y1));
        const int ob = wb / 40, cb = wb % 40;
        if (lane < 8) reinterpret_cast<unsigned*>(p.out)[(ob * 1280 + br * 640 + cb * 16) / 2 + lane] = pack_bf16(y0, y1);
        STAMP(8);
        if (wb == 0 && lane == 0) __builtin_amdgcn_raw_buffer_store_b32(tag, rws, WS_EPOCH + br * 256, 0, SC1);
      }
    }
  }
}

__global__ void __launch_bounds__(NTHR, 1) fused_decode_kernel(Params p) {
  if (blockIdx.x < 128) body<AUX_A>(p); else body<AUX_B>(p);
}

extern "C" void kernel_launch(void* const* d_in, const int* in_sizes, int n_in, void* d_out, int out_size, void* d_ws,
                              size_t ws_size, hipStream_t stream) {
  Params p;
  p.x[0] = d_in[0]; p.k[0] = d_in[1]; p.v[0] = d_in[2]; p.wq[0] = d_in[3]; p.wo[0] = d_in[4];
  p.x[1] = d_in[5]; p.k[1] = d_in[6]; p.v[1] = d_in[7]; p.wq[1] = d_in[8]; p.wo[1] = d_in[9];
  p.out = d_out; p.ws = d_ws;
  constexpr int LDS_BYTES = 64 * 1024;
  fused_decode_kernel<<<256, NTHR, LDS_BYTES, stream>>>(p);
}
```

```cpp
#include <hip/hip_runtime.h>
#include <hip/hip_bf16.h>
#include <stdint.h>
#include <type_traits>
#include <utility>

typedef unsigned u32x4 __attribute__((ext_vector_type(4)));
typedef float f32x4 __attribute__((ext_vector_type(4)));
typedef float f32x2 __attribute__((ext_vector_type(2)));
typedef __attribute__((ext_vector_type(2))) __bf16 bf16x2;
typedef __attribute__((ext_vector_type(8))) __bf16 bf16x8;

constexpr int NT = 2, SC1 = 16;
constexpr int NW = 4, NTHR = (NW + 1) * 64, RING = 32;
constexpr int DEPTH = 8;
constexpr int DEPTH0 = 6;
constexpr int EARLY = 2;
constexpr int RATE = 755;
constexpr unsigned TOFF = 160;
constexpr unsigned TAU2 = (unsigned)(2.0 * 1024.0 * 1024.0 / (RATE * 1e10) * 1e8 * 256.0);
constexpr unsigned SPIN_CAP = 1u << 18;
constexpr int OOB = (int)0x80000000;

constexpr unsigned WS_EPOCH = 0;
constexpr unsigned WGBLK = 12288;
constexpr unsigned WS_QG = 4096;
constexpr unsigned WS_PG = WS_QG + 2048;
constexpr unsigned WS_YG = WS_PG + 4096;
constexpr unsigned WS_ML = WS_YG + 4096;
constexpr unsigned WS_END = WS_QG + 256 * WGBLK;

constexpr int L_RED = 0;
constexpr int L_ACC = 8192;
constexpr int L_QS = 16384;
constexpr int L_OS = L_QS + 1024;
constexpr int L_ML = L_OS + 512;
constexpr int L_AF = L_ML + 256;
constexpr int L_QF = L_AF + 64;
constexpr int L_OF = L_QF + 4;
constexpr int L_TAG = L_OF + 4;
constexpr int L_CLR = 84;
constexpr int LDS_BYTES = 20 * 1024;

struct Params {
  const void* x[2]; const void* k[2]; const void* v[2]; const void* wq[2]; const void* wo[2];
  void* out; void* ws;
};

#define SB() __builtin_amdgcn_sched_barrier(0)
#define MK_RSRC(ptr, bytes) __builtin_amdgcn_make_buffer_rsrc((void*)(ptr), 0, (bytes), 0x00020000)
#define MFMA(a, b, c) __builtin_amdgcn_mfma_f32_16x16x32_bf16((a), __builtin_bit_cast(bf16x8, (b)), (c), 0, 0, 0)
#define IC(n) std::integral_constant<int, (n)>{}
#define LAS __attribute__((address_space(3)))
#define LDSV(T, off) (*reinterpret_cast<volatile LAS T*>(lds + (off)))
#define PIN(x) asm volatile("" : "+v"(x))
constexpr int XOR1 = 0xB1, XOR2 = 0x4E, XOR7 = 0x141, XOR8 = 0x128, ROR4 = 0x124, NEWBCAST = 0x150;

__device__ __forceinline__ float dot2(unsigned k, unsigned q, float acc) {
  return __builtin_amdgcn_fdot2_f32_bf16(__builtin_bit_cast(bf16x2, k), __builtin_bit_cast(bf16x2, q), acc, false);
}
__device__ __forceinline__ float f(unsigned u) { return __builtin_bit_cast(float, u); }
__device__ __forceinline__ unsigned u(float x) { return __builtin_bit_cast(unsigned, x); }
template <int CTL> __device__ __forceinline__ unsigned dppu(unsigned x) { return (unsigned)__builtin_amdgcn_mov_dpp((int)x, CTL, 0xf, 0xf, true); }
template <int CTL> __device__ __forceinline__ float dpp(float x) { return f(dppu<CTL>(u(x))); }
__device__ __forceinline__ unsigned pack_bf16(float lo, float hi) {
  bf16x2 r; r[0] = (__bf16)lo; r[1] = (__bf16)hi;
  return __builtin_bit_cast(unsigned, r);
}
__device__ __forceinline__ float x16_sum(float a) { auto s = __builtin_amdgcn_permlane16_swap(u(a), u(a), false, false); return f(s[0]) + f(s[1]); }
__device__ __forceinline__ float x32_sum(float a) { auto s = __builtin_amdgcn_permlane32_swap(u(a), u(a), false, false); return f(s[0]) + f(s[1]); }
__device__ __forceinline__ float x16_max(float a) { auto s = __builtin_amdgcn_permlane16_swap(u(a), u(a), false, false); return fmaxf(f(s[0]), f(s[1])); }
__device__ __forceinline__ float x32_max(float a) { auto s = __builtin_amdgcn_permlane32_swap(u(a), u(a), false, false); return fmaxf(f(s[0]), f(s[1])); }
__device__ __forceinline__ void wg_barrier() {
  asm volatile("s_waitcnt lgkmcnt(0)" ::: "memory");
  __builtin_amdgcn_s_barrier();
  asm volatile("" ::: "memory");
}
__device__ __forceinline__ void drain() { asm volatile("s_waitcnt vmcnt(0)" ::: "memory"); }
__device__ __forceinline__ bf16x8 sel_frag_pp(unsigned pp, const u32x4& mask) {
  u32x4 a = {pp & mask[0], pp & mask[1], pp & mask[2], pp & mask[3]};
  return __builtin_bit_cast(bf16x8, a);
}
__device__ __forceinline__ bf16x8 sel_frag(unsigned val16, const u32x4& mask) { return sel_frag_pp(val16 * 0x10001u, mask); }
__device__ __forceinline__ unsigned half16(const u32x4& v, int i) { return (i & 1) ? (v[i >> 1] >> 16) : (v[i >> 1] & 0xffffu); }
template <int N> __device__ __forceinline__ void wait_vm() {
  __builtin_amdgcn_s_waitcnt((N & 15) | (7 << 4) | (15 << 8) | ((N >> 4) << 14));
}
template <typename F, int... I> __device__ __forceinline__ void static_for_impl(F&& fn, std::integer_sequence<int, I...>) {
  (fn(std::integral_constant<int, I>{}), ...);
}
template <int N, typename F> __device__ __forceinline__ void static_for(F&& fn) { static_for_impl(fn, std::make_integer_sequence<int, N>{}); }
constexpr int bitrev4(int n) { return ((n & 1) << 3) | ((n & 2) << 1) | ((n & 4) >> 1) | ((n & 8) >> 3); }

template <int N, int SLEEP, typename OFF>
__device__ __forceinline__ void gather(f32x2 (&r)[N], __amdgpu_buffer_rsrc_t rws, OFF&& off, int soff, unsigned tag) {
  int o[N];
#pragma unroll
  for (int k = 0; k < N; ++k) { o[k] = off(k); r[k] = f32x2{0.f, 0.f}; }
  _Pragma("nounroll") for (unsigned spins = 0; spins < SPIN_CAP; ++spins) {
    u32x4 t[N];
#pragma unroll
    for (int k = 0; k < N; ++k) t[k] = __builtin_amdgcn_raw_buffer_load_b128(rws, o[k], soff, SC1);
    bool done = true;
#pragma unroll
    for (int k = 0; k < N; ++k) {
      const bool hit = (o[k] >= 0) & (t[k][1] == tag) & (t[k][3] == tag);
      r[k][0] = hit ? f(t[k][0]) : r[k][0];
      r[k][1] = hit ? f(t[k][2]) : r[k][1];
      o[k] = hit ? OOB : o[k];
      done &= o[k] < 0;
    }
    if (__all(done)) break;
    __builtin_amdgcn_s_sleep(SLEEP);
    asm volatile("" ::: "memory");
  }
}
__device__ __forceinline__ void publish(f32x2 v, __amdgpu_buffer_rsrc_t rws, int off, int soff, unsigned tag) {
  __builtin_amdgcn_raw_buffer_store_b128(u32x4{u(v[0]), tag, u(v[1]), tag}, rws, off, soff, SC1);
}
__device__ __forceinline__ void pace(unsigned dl) {
  asm volatile(".Lpace%=:\n\ts_memrealtime vcc\n\ts_waitcnt lgkmcnt(0)\n\ts_sub_i32 vcc_lo, vcc_lo, %0\n\ts_cmp_lt_i32 vcc_lo, 0\n\ts_cbranch_scc1 .Lpace%="
               :: "s"(dl) : "vcc", "scc");
}

extern "C" __global__ void __launch_bounds__(NTHR, 2) fused_decode_kernel(Params pa) {
  extern __shared__ __attribute__((aligned(16))) unsigned char smem[];
  LAS unsigned char* lds = (LAS unsigned char*)smem;
  Params p = pa;
  SB();
  unsigned code_pf;
  {
    unsigned long long base; unsigned nbytes;
    asm volatile("s_getpc_b64 %0\n.Lpcref:\n\ts_mov_b32 %1, .Lpcref-fused_decode_kernel" : "=s"(base), "=s"(nbytes));
    base -= nbytes;
    asm volatile("s_mov_b32 %0, .Lcode_end-fused_decode_kernel" : "=s"(nbytes));
    code_pf = __builtin_amdgcn_raw_buffer_load_b32(MK_RSRC(base, nbytes), threadIdx.x * 64, 0, 0);
  }
  SB();
  const int bid = (blockIdx.x & 7) * 32 + (blockIdx.x >> 3);
  const int h = bid >> 3, j = bid & 7;
  const int tid = threadIdx.x, lane = tid & 63;
  const int wave = __builtin_amdgcn_readfirstlane(tid >> 6);
  const int g = lane >> 4, c = lane & 15;
  const int gb = j < 5 ? 1 : 0, blk0 = j < 5 ? j : 2 * (j - 5);
  const bool two = j == 5 || j == 6;
  constexpr float C2 = 0.08838834764831845f * 1.4426950408889634f;
  const int box = bid * WGBLK, obox = h * 8 * WGBLK;
  u32x4 mask;
#pragma unroll
  for (int t = 0; t < 4; ++t) mask[t] = (((c & 7) >> 1) == t) ? ((c & 1) ? 0xffff0000u : 0x0000ffffu) : 0u;
  int lo = lane * 16 + wave * 8192;
  int xoff = (c >> 3) * 1280 + ((j & 3) * 160 + 40 * g) * 2, wqoff = ((j & 3) * 160 + 40 * g) * 8192 + c * 16;
  PIN(mask); PIN(lo); PIN(xoff); PIN(wqoff);
  SB();
  asm volatile("" : "+s"(p.x[0]), "+s"(p.x[1]), "+s"(p.k[0]), "+s"(p.k[1]), "+s"(p.v[0]), "+s"(p.v[1]), "+s"(p.wq[0]), "+s"(p.wq[1]),
                    "+s"(p.wo[0]), "+s"(p.wo[1]), "+s"(p.out), "+s"(p.ws));
  unsigned long long t_org = __builtin_amdgcn_s_memrealtime();

  auto rws = MK_RSRC(p.ws, WS_END);

  u32x4 R[RING];
  const int wgbase = bid << 17;
  static_for<EARLY>([&](auto i) {
    constexpr int t = decltype(i)::value;
    R[t] = __builtin_amdgcn_raw_buffer_load_b128(MK_RSRC(p.k[0], wave < NW ? 64 << 20 : 0), lo + (t & 7) * 1024, wgbase + (t >> 3) * 32768, NT);
    SB();
  });

  if (wave < NW) {
    asm volatile("" : "+s"(t_org));
    const unsigned tbase = (unsigned)t_org + TOFF;
    unsigned sched = (EARLY / 2) * TAU2;
    auto issue = [&](auto slot, __amdgpu_buffer_rsrc_t rs, int rbase, auto depth) {
      constexpr int t = decltype(slot)::value;
      if constexpr ((t & 1) == 0) { pace(tbase + (sched >> 8)); sched += TAU2; SB(); }
      wait_vm<decltype(depth)::value - 1>();
      SB();
      R[t] = __builtin_amdgcn_raw_buffer_load_b128(rs, lo + (t & 7) * 1024, rbase + (t >> 3) * 32768, NT);
      SB();
    };
    {
      auto rk = MK_RSRC(p.k[0], 64 << 20);
      static_for<RING - EARLY>([&](auto i) { issue(IC(EARLY + decltype(i)::value), rk, wgbase, IC(DEPTH0)); });
      asm volatile("" :: "v"(code_pf));
    }
    wg_barrier();

    unsigned P[8];
#pragma unroll
    for (int i = 0; i < 8; ++i) P[i] = 0u;
    const bool c8 = c & 8, c4 = c & 4, c2 = c & 2, c1 = c & 1;
    _Pragma("nounroll") for (int it = 0; it < 4; ++it) {
      if (it == 1) {
        _Pragma("nounroll") for (unsigned spins = 0; spins < SPIN_CAP; ++spins) {
          if (__builtin_amdgcn_readfirstlane(LDSV(unsigned, L_QF)) == 1u) break;
          __builtin_amdgcn_s_sleep(1);
        }
      }
      const u32x4 q = LDSV(u32x4, L_QS + it * 256 + 16 * c);
      auto rs = MK_RSRC(it == 3 ? p.v[0] : it == 0 ? p.k[0] : p.k[1], 64 << 20);
      const int rbase = wgbase + ((~it & 1) << 25);
      float l1 = 0.f, l2 = 0.f, l3 = 0.f, l4 = 0.f, s0 = 0.f, s1 = 0.f;
      static_for<RING>([&](auto slot) {
        constexpr int t = decltype(slot)::value;
        float d = dot2(R[t][0], q[0], 0.f);
        d = dot2(R[t][1], q[1], d); d = dot2(R[t][2], q[2], d); d = dot2(R[t][3], q[3], d);
        PIN(d);
        SB();
        issue(slot, rs, rbase, IC(DEPTH));
        d += dpp<XOR8>(d);
        if constexpr ((t & 1) == 0) { l1 = d; PIN(l1); }
        else {
          float e = c8 ? d : l1;
          e += dpp<XOR7>(e);
          if constexpr ((t & 2) == 0) { l2 = e; PIN(l2); }
          else {
            float e2 = c4 ? e : l2;
            e2 += dpp<XOR2>(e2);
            if constexpr ((t & 4) == 0) { l3 = e2; PIN(l3); }
            else {
              float e3 = c2 ? e2 : l3;
              e3 += dpp<XOR1>(e3);
              if constexpr ((t & 8) == 0) { l4 = e3; PIN(l4); }
              else { if constexpr (t < 16) { s0 = c1 ? e3 : l4; PIN(s0); } else { s1 = c1 ? e3 : l4; PIN(s1); } }
            }
          }
        }
        SB();
      });
      float m = fmaxf(s0, s1);
      m = fmaxf(m, dpp<XOR1>(m)); m = fmaxf(m, dpp<XOR2>(m)); m = fmaxf(m, dpp<XOR7>(m)); m = fmaxf(m, dpp<XOR8>(m));
      m = x32_max(x16_max(m));
      const float e0 = __builtin_amdgcn_exp2f((s0 - m) * C2), e1 = __builtin_amdgcn_exp2f((s1 - m) * C2);
      float l = e0 + e1;
      l += dpp<XOR1>(l); l += dpp<XOR2>(l); l += dpp<XOR7>(l); l += dpp<XOR8>(l);
      l = x32_sum(x16_sum(l));
      LDSV(u32x4, L_ML + wave * 64 + it * 16) = u32x4{u(m), u(l), 1u, 0u};
#pragma unroll
      for (int i = 0; i < 6; ++i) P[i] = P[i + 2];
      P[6] = pack_bf16(e0, e0); P[7] = pack_bf16(e1, e1);
    }
    const int acc_off = L_ACC + wave * 512 + c * 32 + (g & 1) * 16;
    _Pragma("nounroll") for (int it = 0; it < 3; ++it) {
      auto rs = MK_RSRC(it == 0 ? p.v[0] : p.v[1], 64 << 20);
      const int rbase = wgbase + ((~it & 1) << 25);
      f32x4 acc = {0.f, 0.f, 0.f, 0.f};
      static_for<RING>([&](auto slot) {
        constexpr int t = decltype(slot)::value;
        PIN(P[t >> 4]);
        acc = MFMA(sel_frag_pp(dppu<NEWBCAST + bitrev4(t & 15)>(P[t >> 4]), mask), R[t], acc);
        PIN(acc);
        SB();
        issue(slot, rs, rbase, IC(DEPTH));
      });
      LDSV(f32x4, acc_off + it * 2048) = acc;
      LDSV(unsigned, L_AF + it * 16 + wave * 4) = 1u;
#pragma unroll
      for (int i = 0; i < 6; ++i) P[i] = P[i + 2];
    }
    {
      auto rwo = MK_RSRC(gb ? p.wo[1] : p.wo[0], 4096 * 640 * 2);
      auto rwo2 = MK_RSRC(gb ? p.wo[1] : p.wo[0], two ? 4096 * 640 * 2 : 0);
      const int wo_off = (h * 128 + 32 * wave + 8 * g) * 1280 + c * 16;
      auto wo_issue = [&](auto kk) {
        constexpr int k = decltype(kk)::value;
        pace(tbase + (sched >> 8)); sched += TAU2; SB();
        R[4 * k] = __builtin_amdgcn_raw_buffer_load_b128(rwo, wo_off + 2 * k * 1280, blk0 * 256, NT);
        R[4 * k + 1] = __builtin_amdgcn_raw_buffer_load_b128(rwo, wo_off + (2 * k + 1) * 1280, blk0 * 256, NT);
        R[4 * k + 2] = __builtin_amdgcn_raw_buffer_load_b128(rwo2, wo_off + 2 * k * 1280, blk0 * 256 + 256, NT);
        R[4 * k + 3] = __builtin_amdgcn_raw_buffer_load_b128(rwo2, wo_off + (2 * k + 1) * 1280, blk0 * 256 + 256, NT);
        SB();
      };
      f32x4 acc = {0.f, 0.f, 0.f, 0.f};
      static_for<RING>([&](auto slot) {
        constexpr int t = decltype(slot)::value;
        PIN(P[t >> 4]);
        acc = MFMA(sel_frag_pp(dppu<NEWBCAST + bitrev4(t & 15)>(P[t >> 4]), mask), R[t], acc);
        PIN(acc);
        SB();
        if constexpr (t == 8 || t == 16 || t == 24) wo_issue(IC(t / 8 - 1));
      });
      LDSV(f32x4, acc_off + 3 * 2048) = acc;
      LDSV(unsigned, L_AF + 3 * 16 + wave * 4) = 1u;
      SB();
      wo_issue(IC(3));
    }
    {
      const unsigned tag = LDSV(unsigned, L_TAG);
      const int go = WS_QG + g * WGBLK + wave * 256 + c * 16;
      _Pragma("nounroll") for (unsigned spins = 0; spins < SPIN_CAP; ++spins)
        if (__builtin_amdgcn_readfirstlane(LDSV(unsigned, L_OF)) == 1u) break;
      f32x2 r[4];
      gather<4, 1>(r, rws, [&](int k) { return go + (k & 1) * 4 * WGBLK + (k >> 1) * 1024; }, obox + (1 - gb) * 2048, tag + gb);
      const f32x2 a = r[0] + r[1], b = r[2] + r[3];
      const float a0 = x32_sum(x16_sum(a[0])), a1 = x32_sum(x16_sum(a[1]));
      const float b0 = x32_sum(x16_sum(b[0])), b1 = x32_sum(x16_sum(b[1]));
      LDSV(unsigned, L_OS + wave * 128 + (g & 1) * 64 + c * 4) = (g & 1) ? pack_bf16(b0, b1) : pack_bf16(a0, a1);
      const u32x4 ov = LDSV(u32x4, L_OS + wave * 128 + (c >> 3) * 64 + 16 * g);
      f32x4 y = {0.f, 0.f, 0.f, 0.f}, z = {0.f, 0.f, 0.f, 0.f};
#pragma unroll
      for (int i = 0; i < 8; ++i) {
        const bf16x8 af = sel_frag(half16(ov, i), mask);
        y = MFMA(af, R[4 * (i >> 1) + (i & 1)], y);
        z = MFMA(af, R[4 * (i >> 1) + 2 + (i & 1)], z);
      }
      LDSV(f32x4, L_RED + wave * 1024 + (g >> 1) * 512 + 32 * c + 16 * (g & 1)) = y;
      LDSV(f32x4, L_RED + 4096 + wave * 1024 + (g >> 1) * 512 + 32 * c + 16 * (g & 1)) = z;
    }
    asm volatile(".Lcode_end:");
    wg_barrier();
  } else {
    __builtin_amdgcn_s_setprio(3);
    auto rx = MK_RSRC(j & 4 ? p.x[1] : p.x[0], 2 * 640 * 2);
    auto rwq = MK_RSRC(j & 4 ? p.wq[1] : p.wq[0], 640 * 4096 * 2);
    u32x4 xa[5], wq[40];
#pragma unroll
    for (int i = 0; i < 5; ++i) xa[i] = __builtin_amdgcn_raw_buffer_load_b128(rx, xoff + i * 16, 0, 0);
    SB();
#pragma unroll
    for (int i = 0; i < 40; ++i) { wq[i] = __builtin_amdgcn_raw_buffer_load_b128(rwq, wqoff, h * 256 + i * 8192, NT); SB(); }
    const unsigned epoch = __builtin_amdgcn_raw_buffer_load_b32(rws, WS_EPOCH, 0, SC1);
    SB();
    LDSV(unsigned, L_ML + lane * 4) = 0u;
    if (lane < L_CLR - 64) LDSV(unsigned, L_ML + 256 + lane * 4) = 0u;
    f32x4 qa = {0.f, 0.f, 0.f, 0.f};
    static_for<40>([&](auto i) {
      constexpr int I = decltype(i)::value;
      qa = MFMA(sel_frag(half16(xa[I >> 3], I & 7), mask), wq[I], qa);
      PIN(qa);
      SB();
    });
    asm volatile("" :: "v"(code_pf));
    const unsigned tag = epoch + 1u;
    publish(f32x2{qa[0], qa[1]}, rws, WS_QG + lane * 16, box, tag);
    publish(f32x2{qa[2], qa[3]}, rws, WS_QG + 1024 + lane * 16, box, tag);
    LDSV(unsigned, L_TAG) = tag;
    drain();
    const int qoff = WS_QG + (lane & 1) * 1024 + (lane & 2) * 128 + (lane >> 2) * 16;
    {
      f32x2 t[4];
      gather<4, 1>(t, rws, [&](int k) { return qoff + k * WGBLK; }, obox, tag);
      const f32x2 q = (t[0] + t[1]) + (t[2] + t[3]);
      LDSV(unsigned, L_QS + lane * 4) = pack_bf16(q[0], q[1]);
    }
    wg_barrier();
    {
      f32x2 t[12];
      gather<12, 1>(t, rws, [&](int k) { return qoff + (k & 3) * WGBLK + (((k >> 2) + 1) >> 1) * 4 * WGBLK + (((k >> 2) + 1) & 1) * 512; }, obox, tag);
#pragma unroll
      for (int r = 1; r < 4; ++r) {
        const f32x2 q = (t[4 * r - 4] + t[4 * r - 3]) + (t[4 * r - 2] + t[4 * r - 1]);
        LDSV(unsigned, L_QS + r * 256 + lane * 4) = pack_bf16(q[0], q[1]);
      }
    }
    LDSV(unsigned, L_QF) = 1u;

    auto wait_flags = [&](int flag_off, auto sleep) {
      _Pragma("nounroll") for (unsigned spins = 0; spins < SPIN_CAP; ++spins) {
        if (__all(LDSV(unsigned, flag_off) == 1u)) break;
        __builtin_amdgcn_s_sleep(decltype(sleep)::value);
      }
    };
    wait_flags(L_ML + c * 16 + 8, IC(8));
    float wt;
    {
      const u32x4 e = LDSV(u32x4, L_ML + c * 16);
      const float mw = f(e[0]);
      float M = fmaxf(mw, dpp<ROR4>(mw)); M = fmaxf(M, dpp<XOR8>(M));
      float L = __builtin_amdgcn_exp2f((mw - M) * C2) * f(e[1]); L += dpp<ROR4>(L); L += dpp<XOR8>(L);
      publish(f32x2{M, L}, rws, lane < 4 ? (int)(WS_ML + lane * 16) : OOB, box, tag);
      unsigned touch = __builtin_amdgcn_raw_buffer_load_b32(MK_RSRC(gb ? p.wo[1] : p.wo[0], 4096 * 640 * 2), lane < 4 ? (h * 128 + lane * 32) * 1280 + blk0 * 256 : OOB, 0, NT);
      asm volatile("" :: "v"(touch));
      drain();
      f32x2 t[1];
      gather<1, 32>(t, rws, [&](int) { return WS_ML + ((lane >> 2) & 7) * WGBLK + (lane & 3) * 16; }, obox, tag);
      const float mj = t[0][0];
      float MM = fmaxf(mj, dpp<ROR4>(mj)); MM = fmaxf(MM, dpp<XOR8>(MM)); MM = x16_max(MM);
      float LL = __builtin_amdgcn_exp2f((mj - MM) * C2) * t[0][1]; LL += dpp<ROR4>(LL); LL += dpp<XOR8>(LL); LL = x16_sum(LL);
      wt = __builtin_amdgcn_exp2f((mw - MM) * C2) / LL;
    }
    auto share = [&](int r) {
      f32x2 o2 = {0.f, 0.f};
#pragma unroll
      for (int i = 0; i < 4; ++i) o2 += f(__builtin_amdgcn_readlane(u(wt), i * 4 + r)) * LDSV(f32x2, L_ACC + r * 2048 + i * 512 + lane * 8);
      return o2;
    };
    wait_flags(L_AF + (lane & 7) * 4, IC(8));
    {
      const f32x2 o0 = share(0), o1 = share(1);
      wait_flags(L_AF + 32 + (lane & 3) * 4, IC(4));
      publish(share(2), rws, WS_QG + lane * 16, box, tag + 1u);
      __builtin_amdgcn_s_sleep(20);
      publish(o0, rws, WS_PG + lane * 16, box, tag);
      publish(o1, rws, WS_PG + 1024 + lane * 16, box, tag);
    }
    wait_flags(L_AF + 48 + (lane & 3) * 4, IC(1));
    publish(share(3), rws, WS_QG + 1024 + lane * 16, box, tag + 1u);
    drain();
    LDSV(unsigned, L_OF) = 1u;
    wg_barrier();
#pragma unroll
    for (int blk = 0; blk < 2; ++blk) {
      if (blk == 1 && !two) break;
      const int src = L_RED + blk * 4096 + lane * 8;
      f32x2 sa = LDSV(f32x2, src), sb = LDSV(f32x2, src + 512);
#pragma unroll
      for (int w = 1; w < NW; ++w) { sa += LDSV(f32x2, src + w * 1024); sb += LDSV(f32x2, src + w * 1024 + 512); }
      publish(sa, rws, WS_YG + blk * 2048 + lane * 16, box, tag);
      publish(sb, rws, WS_YG + blk * 2048 + 1024 + lane * 16, box, tag);
    }
    const int rbr = 1 - gb, ri = gb ? h * 5 + j : h * 3 + (j - 5);
    if (ri < 80) {
      const int ob = ri / 40, cb = ri % 40, b128 = cb >> 3;
      const int src = rbr ? b128 * WGBLK : (5 + (b128 >> 1)) * WGBLK + (b128 & 1) * 2048;
      f32x2 r[4];
      gather<4, 1>(r, rws, [&](int k) { return WS_YG + ((lane >> 3) + 8 * k) * 8 * WGBLK + (lane & 7) * 16; }, src + ob * 1024 + (cb & 7) * 128, tag);
      const f32x2 y = (r[0] + r[1]) + (r[2] + r[3]);
      float y0 = y[0], y1 = y[1];
      y0 += dpp<XOR8>(y0); y1 += dpp<XOR8>(y1);
      y0 = x32_sum(x16_sum(y0)); y1 = x32_sum(x16_sum(y1));
      __builtin_amdgcn_raw_buffer_store_b32(pack_bf16(y0, y1), MK_RSRC(p.out, 5120), lane < 8 ? lane * 4 : OOB, (ob * 1280 + rbr * 640 + cb * 16) * 2, 0);
      if (bid == 5) __builtin_amdgcn_raw_buffer_store_b32(tag + 1u, rws, lane == 0 ? (int)WS_EPOCH : OOB, 0, SC1);
    }
  }
}

extern "C" void kernel_launch(void* const* d_in, const int* in_sizes, int n_in, void* d_out, int out_size, void* d_ws,
                              size_t ws_size, hipStream_t stream) {
  Params p;
  p.x[0] = d_in[0]; p.k[0] = d_in[1]; p.v[0] = d_in[2]; p.wq[0] = d_in[3]; p.wo[0] = d_in[4];
  p.x[1] = d_in[5]; p.k[1] = d_in[6]; p.v[1] = d_in[7]; p.wq[1] = d_in[8]; p.wo[1] = d_in[9];
  p.out = d_out; p.ws = d_ws;
  fused_decode_kernel<<<256, NTHR, LDS_BYTES, stream>>>(p);
}
```

```cpp
#include <hip/hip_runtime.h>
#include <hip/hip_bf16.h>
#include <stdint.h>
#include <type_traits>
#include <utility>

typedef unsigned u32x4 __attribute__((ext_vector_type(4)));
typedef float f32x4 __attribute__((ext_vector_type(4)));
typedef float f32x2 __attribute__((ext_vector_type(2)));
typedef __attribute__((ext_vector_type(2))) __bf16 bf16x2;
typedef __attribute__((ext_vector_type(8))) __bf16 bf16x8;

constexpr int NT = 2, SC1 = 16;
constexpr int NW = 4, NTHR = (NW + 1) * 64, RING = 32;
constexpr int DEPTH = 12;
constexpr int DEPTH0 = 8;
constexpr int EARLY = 2;
constexpr int RATE0 = 741, RATE7 = 790;
constexpr unsigned tau2(int rate) { return (unsigned)(2.0 * 1024.0 * 1024.0 / (rate * 1e10) * 1e8 * 256.0); }
constexpr unsigned TAU2 = tau2(RATE0), DTAU2 = (tau2(RATE0) - tau2(RATE7)) / 7, TAU2_WO = tau2(755);
constexpr unsigned TOFF = 150;
constexpr unsigned SPIN_CAP = 1u << 18;
constexpr int OOB = (int)0x80000000;

constexpr unsigned WS_EPOCH = 0;
constexpr unsigned WGBLK = 4352;
constexpr unsigned WS_QG = 4096;
constexpr unsigned WS_PG = WS_QG + 2048;
constexpr unsigned WS_ML = WS_PG + 2048;
constexpr unsigned WS_YG = WS_QG + 8 * WGBLK;
constexpr unsigned OBLK = 8 * WGBLK + 10 * 2048;
constexpr unsigned WS_END = WS_QG + 32 * OBLK;

constexpr int L_RED = 0;
constexpr int L_ACC = 8192;
constexpr int L_QS = 16384;
constexpr int L_OS = L_QS + 1024;
constexpr int L_ML = L_OS + 512;
constexpr int L_AF = L_ML + 256;
constexpr int L_QF = L_AF + 64;
constexpr int L_OF = L_QF + 4;
constexpr int L_TAG = L_OF + 4;
constexpr int L_CLR = 84;
constexpr int LDS_BYTES = 20 * 1024;

struct Params {
  const void* x[2]; const void* k[2]; const void* v[2]; const void* wq[2]; const void* wo[2];
  void* out; void* ws;
};

#define SB() __builtin_amdgcn_sched_barrier(0)
#define MK_RSRC(ptr, bytes) __builtin_amdgcn_make_buffer_rsrc((void*)(ptr), 0, (bytes), 0x00020000)
#define MFMA(a, b, c) __builtin_amdgcn_mfma_f32_16x16x32_bf16((a), __builtin_bit_cast(bf16x8, (b)), (c), 0, 0, 0)
#define IC(n) std::integral_constant<int, (n)>{}
#define LAS __attribute__((address_space(3)))
#define LDSV(T, off) (*reinterpret_cast<volatile LAS T*>(lds + (off)))
#define PIN(x) asm volatile("" : "+v"(x))
constexpr int XOR1 = 0xB1, XOR2 = 0x4E, XOR7 = 0x141, XOR8 = 0x128, ROR4 = 0x124, NEWBCAST = 0x150;

__device__ __forceinline__ float dot2(unsigned k, unsigned q, float acc) {
  return __builtin_amdgcn_fdot2_f32_bf16(__builtin_bit_cast(bf16x2, k), __builtin_bit_cast(bf16x2, q), acc, false);
}
__device__ __forceinline__ float f(unsigned u) { return __builtin_bit_cast(float, u); }
__device__ __forceinline__ unsigned u(float x) { return __builtin_bit_cast(unsigned, x); }
template <int CTL> __device__ __forceinline__ unsigned dppu(unsigned x) { return (unsigned)__builtin_amdgcn_mov_dpp((int)x, CTL, 0xf, 0xf, true); }
template <int CTL> __device__ __forceinline__ float dpp(float x) { return f(dppu<CTL>(u(x))); }
__device__ __forceinline__ unsigned pack_bf16(float lo, float hi) {
  bf16x2 r; r[0] = (__bf16)lo; r[1] = (__bf16)hi;
  return __builtin_bit_cast(unsigned, r);
}
__device__ __forceinline__ float x16_sum(float a) { auto s = __builtin_amdgcn_permlane16_swap(u(a), u(a), false, false); return f(s[0]) + f(s[1]); }
__device__ __forceinline__ float x32_sum(float a) { auto s = __builtin_amdgcn_permlane32_swap(u(a), u(a), false, false); return f(s[0]) + f(s[1]); }
__device__ __forceinline__ float x16_max(float a) { auto s = __builtin_amdgcn_permlane16_swap(u(a), u(a), false, false); return fmaxf(f(s[0]), f(s[1])); }
__device__ __forceinline__ float x32_max(float a) { auto s = __builtin_amdgcn_permlane32_swap(u(a), u(a), false, false); return fmaxf(f(s[0]), f(s[1])); }
__device__ __forceinline__ void wg_barrier() {
  asm volatile("s_waitcnt lgkmcnt(0)" ::: "memory");
  __builtin_amdgcn_s_barrier();
  asm volatile("" ::: "memory");
}
__device__ __forceinline__ void drain() { asm volatile("s_waitcnt vmcnt(0)" ::: "memory"); }
__device__ __forceinline__ bf16x8 sel_frag_pp(unsigned pp, const u32x4& mask) {
  u32x4 a = {pp & mask[0], pp & mask[1], pp & mask[2], pp & mask[3]};
  return __builtin_bit_cast(bf16x8, a);
}
__device__ __forceinline__ bf16x8 sel_frag(unsigned val16, const u32x4& mask) { return sel_frag_pp(val16 * 0x10001u, mask); }
__device__ __forceinline__ unsigned half16(const u32x4& v, int i) { return (i & 1) ? (v[i >> 1] >> 16) : (v[i >> 1] & 0xffffu); }
template <int N> __device__ __forceinline__ void wait_vm() {
  __builtin_amdgcn_s_waitcnt((N & 15) | (7 << 4) | (15 << 8) | ((N >> 4) << 14));
}
template <typename F, int... I> __device__ __forceinline__ void static_for_impl(F&& fn, std::integer_sequence<int, I...>) {
  (fn(std::integral_constant<int, I>{}), ...);
}
template <int N, typename F> __device__ __forceinline__ void static_for(F&& fn) { static_for_impl(fn, std::make_integer_sequence<int, N>{}); }
constexpr int bitrev4(int n) { return ((n & 1) << 3) | ((n & 2) << 1) | ((n & 4) >> 1) | ((n & 8) >> 3); }

template <int N, int SLEEP, typename OFF>
__device__ __forceinline__ void gather(f32x2 (&r)[N], __amdgpu_buffer_rsrc_t rws, OFF&& off, int soff, unsigned tag) {
  int o[N];
#pragma unroll
  for (int k = 0; k < N; ++k) { o[k] = off(k); r[k] = f32x2{0.f, 0.f}; }
  _Pragma("nounroll") for (unsigned spins = 0; spins < SPIN_CAP; ++spins) {
    u32x4 t[N];
#pragma unroll
    for (int k = 0; k < N; ++k) t[k] = __builtin_amdgcn_raw_buffer_load_b128(rws, o[k], soff, SC1);
    bool done = true;
#pragma unroll
    for (int k = 0; k < N; ++k) {
      const bool hit = (o[k] >= 0) & (t[k][1] == tag) & (t[k][3] == tag);
      r[k][0] = hit ? f(t[k][0]) : r[k][0];
      r[k][1] = hit ? f(t[k][2]) : r[k][1];
      o[k] = hit ? OOB : o[k];
      done &= o[k] < 0;
    }
    if (__all(done)) break;
    __builtin_amdgcn_s_sleep(SLEEP);
    asm volatile("" ::: "memory");
  }
}
__device__ __forceinline__ void publish(f32x2 v, __amdgpu_buffer_rsrc_t rws, int off, int soff, unsigned tag) {
  __builtin_amdgcn_raw_buffer_store_b128(u32x4{u(v[0]), tag, u(v[1]), tag}, rws, off, soff, SC1);
}
__device__ __forceinline__ void pace(unsigned dl) {
  asm volatile(".Lpace%=:\n\ts_memrealtime vcc\n\ts_waitcnt lgkmcnt(0)\n\ts_sub_i32 vcc_lo, vcc_lo, %0\n\ts_cmp_lt_i32 vcc_lo, 0\n\ts_cbranch_scc1 .Lpace%="
               :: "s"(dl) : "vcc", "scc");
}

extern "C" __global__ void __launch_bounds__(NTHR, 2) fused_decode_kernel(Params pa) {
  extern __shared__ __attribute__((aligned(16))) unsigned char smem[];
  LAS unsigned char* lds = (LAS unsigned char*)smem;
  Params p = pa;
  SB();
  unsigned code_pf;
  {
    unsigned long long base; unsigned nbytes;
    asm volatile("s_getpc_b64 %0\n.Lpcref:\n\ts_mov_b32 %1, .Lpcref-fused_decode_kernel" : "=s"(base), "=s"(nbytes));
    base -= nbytes;
    asm volatile("s_mov_b32 %0, .Lcode_end-fused_decode_kernel" : "=s"(nbytes));
    code_pf = __builtin_amdgcn_raw_buffer_load_b32(MK_RSRC(base, nbytes), threadIdx.x * 64, 0, 0);
  }
  SB();
  const int bid = (blockIdx.x & 7) * 32 + (blockIdx.x >> 3);
  const int h = bid >> 3, j = bid & 7;
  const int tid = threadIdx.x, lane = tid & 63;
  const int wave = __builtin_amdgcn_readfirstlane(tid >> 6);
  const int g = lane >> 4, c = lane & 15;
  const int gb = j < 5 ? 1 : 0, blk0 = j < 5 ? j : 2 * (j - 5);
  const bool two = j == 5 || j == 6;
  constexpr float C2 = 0.08838834764831845f * 1.4426950408889634f;
  const int obox = h * OBLK, box = obox + j * WGBLK;
  u32x4 mask;
#pragma unroll
  for (int t = 0; t < 4; ++t) mask[t] = (((c & 7) >> 1) == t) ? ((c & 1) ? 0xffff0000u : 0x0000ffffu) : 0u;
  int lo = lane * 16 + wave * 8192;
  int xoff = (c >> 3) * 1280 + ((j & 3) * 160 + 40 * g) * 2, wqoff = ((j & 3) * 160 + 40 * g) * 8192 + c * 16;
  PIN(mask); PIN(lo); PIN(xoff); PIN(wqoff);
  SB();
  asm volatile("" : "+s"(p.x[0]), "+s"(p.x[1]), "+s"(p.k[0]), "+s"(p.k[1]), "+s"(p.v[0]), "+s"(p.v[1]), "+s"(p.wq[0]), "+s"(p.wq[1]),
                    "+s"(p.wo[0]), "+s"(p.wo[1]), "+s"(p.out), "+s"(p.ws));
  unsigned long long t_org = __builtin_amdgcn_s_memrealtime();

  auto rws = MK_RSRC(p.ws, WS_END);

  u32x4 R[RING];
  const int wgbase = bid << 17;
  static_for<EARLY>([&](auto i) {
    constexpr int t = decltype(i)::value;
    R[t] = __builtin_amdgcn_raw_buffer_load_b128(MK_RSRC(p.k[0], wave < NW ? 64 << 20 : 0), lo + (t & 7) * 1024, wgbase + (t >> 3) * 32768, NT);
    SB();
  });

  if (wave < NW) {
    asm volatile("" : "+s"(t_org));
    const unsigned tbase = (unsigned)t_org + TOFF + wave * 7;
    unsigned sched = (EARLY / 2) * TAU2, tau = TAU2;
    auto issue = [&](auto slot, __amdgpu_buffer_rsrc_t rs, int rbase, auto depth) {
      constexpr int t = decltype(slot)::value;
      if constexpr ((t & 1) == 0) { pace(tbase + (sched >> 8)); sched += tau; SB(); }
      wait_vm<decltype(depth)::value - 1>();
      SB();
      R[t] = __builtin_amdgcn_raw_buffer_load_b128(rs, lo + (t & 7) * 1024, rbase + (t >> 3) * 32768, NT);
      SB();
    };
    {
      auto rk = MK_RSRC(p.k[0], 64 << 20);
      static_for<RING - EARLY>([&](auto i) { issue(IC(EARLY + decltype(i)::value), rk, wgbase, IC(DEPTH0)); });
      asm volatile("" :: "v"(code_pf));
    }
    wg_barrier();

    unsigned P[8];
#pragma unroll
    for (int i = 0; i < 8; ++i) P[i] = 0u;
    const bool c8 = c & 8, c4 = c & 4, c2 = c & 2, c1 = c & 1;
    _Pragma("nounroll") for (int it = 0; it < 4; ++it) {
      if (it == 1) {
        _Pragma("nounroll") for (unsigned spins = 0; spins < SPIN_CAP; ++spins) {
          if (__builtin_amdgcn_readfirstlane(LDSV(unsigned, L_QF)) == 1u) break;
          __builtin_amdgcn_s_sleep(1);
        }
      }
      tau -= DTAU2;
      const u32x4 q = LDSV(u32x4, L_QS + it * 256 + 16 * c);
      auto rs = MK_RSRC(it == 3 ? p.v[0] : it == 0 ? p.k[0] : p.k[1], 64 << 20);
      const int rbase = wgbase + ((~it & 1) << 25);
      float l1 = 0.f, l2 = 0.f, l3 = 0.f, l4 = 0.f, s0 = 0.f, s1 = 0.f;
      static_for<RING>([&](auto slot) {
        constexpr int t = decltype(slot)::value;
        float d = dot2(R[t][0], q[0], 0.f);
        d = dot2(R[t][1], q[1], d); d = dot2(R[t][2], q[2], d); d = dot2(R[t][3], q[3], d);
        PIN(d);
        SB();
        issue(slot, rs, rbase, IC(DEPTH));
        d += dpp<XOR8>(d);
        if constexpr ((t & 1) == 0) { l1 = d; PIN(l1); }
        else {
          float e = c8 ? d : l1;
          e += dpp<XOR7>(e);
          if constexpr ((t & 2) == 0) { l2 = e; PIN(l2); }
          else {
            float e2 = c4 ? e : l2;
            e2 += dpp<XOR2>(e2);
            if constexpr ((t & 4) == 0) { l3 = e2; PIN(l3); }
            else {
              float e3 = c2 ? e2 : l3;
              e3 += dpp<XOR1>(e3);
              if constexpr ((t & 8) == 0) { l4 = e3; PIN(l4); }
              else { if constexpr (t < 16) { s0 = c1 ? e3 : l4; PIN(s0); } else { s1 = c1 ? e3 : l4; PIN(s1); } }
            }
          }
        }
        SB();
      });
      float m = fmaxf(s0, s1);
      m = fmaxf(m, dpp<XOR1>(m)); m = fmaxf(m, dpp<XOR2>(m)); m = fmaxf(m, dpp<XOR7>(m)); m = fmaxf(m, dpp<XOR8>(m));
      m = x32_max(x16_max(m));
      const float e0 = __builtin_amdgcn_exp2f((s0 - m) * C2), e1 = __builtin_amdgcn_exp2f((s1 - m) * C2);
      float l = e0 + e1;
      l += dpp<XOR1>(l); l += dpp<XOR2>(l); l += dpp<XOR7>(l); l += dpp<XOR8>(l);
      l = x32_sum(x16_sum(l));
      LDSV(u32x4, L_ML + wave * 64 + it * 16) = u32x4{u(m), u(l), 1u, 0u};
#pragma unroll
      for (int i = 0; i < 6; ++i) P[i] = P[i + 2];
      P[6] = pack_bf16(e0, e0); P[7] = pack_bf16(e1, e1);
    }
    const int acc_off = L_ACC + wave * 512 + c * 32 + (g & 1) * 16;
    _Pragma("nounroll") for (int it = 0; it < 3; ++it) {
      tau -= DTAU2;
      auto rs = MK_RSRC(it == 0 ? p.v[0] : p.v[1], 64 << 20);
      const int rbase = wgbase + ((~it & 1) << 25);
      f32x4 acc = {0.f, 0.f, 0.f, 0.f};
      static_for<RING>([&](auto slot) {
        constexpr int t = decltype(slot)::value;
        PIN(P[t >> 4]);
        acc = MFMA(sel_frag_pp(dppu<NEWBCAST + bitrev4(t & 15)>(P[t >> 4]), mask), R[t], acc);
        PIN(acc);
        SB();
        issue(slot, rs, rbase, IC(DEPTH));
      });
      LDSV(f32x4, acc_off + it * 2048) = acc;
      LDSV(unsigned, L_AF + it * 16 + wave * 4) = 1u;
#pragma unroll
      for (int i = 0; i < 6; ++i) P[i] = P[i + 2];
    }
    {
      auto rwo = MK_RSRC(gb ? p.wo[1] : p.wo[0], 4096 * 640 * 2);
      auto rwo2 = MK_RSRC(gb ? p.wo[1] : p.wo[0], two ? 4096 * 640 * 2 : 0);
      const int wo_off = (h * 128 + 32 * wave + 8 * g) * 1280 + c * 16;
      auto wo_issue = [&](auto kk) {
        constexpr int k = decltype(kk)::value;
        pace(tbase + (sched >> 8)); sched += TAU2_WO; SB();
        R[4 * k] = __builtin_amdgcn_raw_buffer_load_b128(rwo, wo_off + 2 * k * 1280, blk0 * 256, NT);
        R[4 * k + 1] = __builtin_amdgcn_raw_buffer_load_b128(rwo, wo_off + (2 * k + 1) * 1280, blk0 * 256, NT);
        R[4 * k + 2] = __builtin_amdgcn_raw_buffer_load_b128(rwo2, wo_off + 2 * k * 1280, blk0 * 256 + 256, NT);
        R[4 * k + 3] = __builtin_amdgcn_raw_buffer_load_b128(rwo2, wo_off + (2 * k + 1) * 1280, blk0 * 256 + 256, NT);
        SB();
      };
      f32x4 acc = {0.f, 0.f, 0.f, 0.f};
      static_for<RING>([&](auto slot) {
        constexpr int t = decltype(slot)::value;
        PIN(P[t >> 4]);
        acc = MFMA(sel_frag_pp(dppu<NEWBCAST + bitrev4(t & 15)>(P[t >> 4]), mask), R[t], acc);
        PIN(acc);
        SB();
        if constexpr (t == 8 || t == 16 || t == 24) wo_issue(IC(t / 8 - 1));
      });
      LDSV(f32x4, acc_off + 3 * 2048) = acc;
      LDSV(unsigned, L_AF + 3 * 16 + wave * 4) = 1u;
      SB();
      wo_issue(IC(3));
    }
    {
      const unsigned tag = LDSV(unsigned, L_TAG);
      const int go = WS_QG + g * WGBLK + wave * 256 + c * 16;
      _Pragma("nounroll") for (unsigned spins = 0; spins < SPIN_CAP; ++spins)
        if (__builtin_amdgcn_readfirstlane(LDSV(unsigned, L_OF)) == 1u) break;
      f32x2 r[4];
      gather<4, 1>(r, rws, [&](int k) { return go + (k & 1) * 4 * WGBLK + (k >> 1) * 1024; }, obox + (1 - gb) * 2048, tag + gb);
      const f32x2 a = r[0] + r[1], b = r[2] + r[3];
      const float a0 = x32_sum(x16_sum(a[0])), a1 = x32_sum(x16_sum(a[1]));
      const float b0 = x32_sum(x16_sum(b[0])), b1 = x32_sum(x16_sum(b[1]));
      LDSV(unsigned, L_OS + wave * 128 + (g & 1) * 64 + c * 4) = (g & 1) ? pack_bf16(b0, b1) : pack_bf16(a0, a1);
      const u32x4 ov = LDSV(u32x4, L_OS + wave * 128 + (c >> 3) * 64 + 16 * g);
      f32x4 y = {0.f, 0.f, 0.f, 0.f}, z = {0.f, 0.f, 0.f, 0.f};
#pragma unroll
      for (int i = 0; i < 8; ++i) {
        const bf16x8 af = sel_frag(half16(ov, i), mask);
        y = MFMA(af, R[4 * (i >> 1) + (i & 1)], y);
        z = MFMA(af, R[4 * (i >> 1) + 2 + (i & 1)], z);
      }
      LDSV(f32x4, L_RED + wave * 1024 + (g >> 1) * 512 + 32 * c + 16 * (g & 1)) = y;
      LDSV(f32x4, L_RED + 4096 + wave * 1024 + (g >> 1) * 512 + 32 * c + 16 * (g & 1)) = z;
    }
    asm volatile(".Lcode_end:");
    wg_barrier();
  } else {
    __builtin_amdgcn_s_setprio(3);
    auto rx = MK_RSRC(j & 4 ? p.x[1] : p.x[0], 2 * 640 * 2);
    auto rwq = MK_RSRC(j & 4 ? p.wq[1] : p.wq[0], 640 * 4096 * 2);
    u32x4 xa[5], wq[40];
#pragma unroll
    for (int i = 0; i < 5; ++i) xa[i] = __builtin_amdgcn_raw_buffer_load_b128(rx, xoff + i * 16, 0, 0);
    SB();
#pragma unroll
    for (int i = 0; i < 40; ++i) { wq[i] = __builtin_amdgcn_raw_buffer_load_b128(rwq, wqoff, h * 256 + i * 8192, NT); SB(); }
    const unsigned epoch = __builtin_amdgcn_raw_buffer_load_b32(rws, WS_EPOCH, 0, SC1);
    SB();
    LDSV(unsigned, L_ML + lane * 4) = 0u;
    if (lane < L_CLR - 64) LDSV(unsigned, L_ML + 256 + lane * 4) = 0u;
    f32x4 qa = {0.f, 0.f, 0.f, 0.f};
    static_for<40>([&](auto i) {
      constexpr int I = decltype(i)::value;
      qa = MFMA(sel_frag(half16(xa[I >> 3], I & 7), mask), wq[I], qa);
      PIN(qa);
      SB();
    });
    asm volatile("" :: "v"(code_pf));
    const unsigned tag = epoch + 1u;
    publish(f32x2{qa[0], qa[1]}, rws, WS_QG + lane * 16, box, tag);
    publish(f32x2{qa[2], qa[3]}, rws, WS_QG + 1024 + lane * 16, box, tag);
    LDSV(unsigned, L_TAG) = tag;
    drain();
    const int qoff = WS_QG + (lane & 1) * 1024 + (lane & 2) * 128 + (lane >> 2) * 16;
    {
      f32x2 t[4];
      gather<4, 1>(t, rws, [&](int k) { return qoff + k * WGBLK; }, obox, tag);
      const f32x2 q = (t[0] + t[1]) + (t[2] + t[3]);
      LDSV(unsigned, L_QS + lane * 4) = pack_bf16(q[0], q[1]);
    }
    unsigned touch1 = __builtin_amdgcn_raw_buffer_load_b32(MK_RSRC(p.k[0], 64 << 20), lane == 0 ? 0 : OOB, wgbase + (1 << 25), NT);
    wg_barrier();
    asm volatile("" :: "v"(touch1));
    {
      f32x2 t[12];
      gather<12, 1>(t, rws, [&](int k) { return qoff + (k & 3) * WGBLK + (((k >> 2) + 1) >> 1) * 4 * WGBLK + (((k >> 2) + 1) & 1) * 512; }, obox, tag);
#pragma unroll
      for (int r = 1; r < 4; ++r) {
        const f32x2 q = (t[4 * r - 4] + t[4 * r - 3]) + (t[4 * r - 2] + t[4 * r - 1]);
        LDSV(unsigned, L_QS + r * 256 + lane * 4) = pack_bf16(q[0], q[1]);
      }
    }
    LDSV(unsigned, L_QF) = 1u;

    const int rbr = 1 - gb, ri = gb ? h * 5 + j : h * 3 + (j - 5);
    const int ob = ri / 40, cb = ri % 40;
    const int yoff = WS_YG + (lane >> 3) * OBLK + (lane & 7) * 16, ysrc = (rbr * 5 + (cb >> 3)) * 2048 + (ob & 1) * 1024 + (cb & 7) * 128;
    {
      _Pragma("nounroll") for (int r = 2; r < 8; ++r) {
        const void* base = r < 4 ? p.k[1] : r < 6 ? p.v[0] : p.v[1];
        unsigned t1 = __builtin_amdgcn_raw_buffer_load_b32(MK_RSRC(base, 64 << 20), lane == 0 ? 0 : OOB, wgbase + ((r & 1) << 25), NT);
        asm volatile("" :: "v"(t1));
        __builtin_amdgcn_s_sleep(32);
      }
      __builtin_amdgcn_raw_buffer_store_b32(0u, MK_RSRC(p.out, 5120), lane < 8 && ri < 80 ? lane * 4 : OOB, (ob * 1280 + rbr * 640 + cb * 16) * 2, SC1);
      if (bid == 5) __builtin_amdgcn_raw_buffer_store_b32(epoch, rws, lane == 0 ? (int)WS_EPOCH : OOB, 0, SC1);
    }
    auto wait_flags = [&](int flag_off, auto sleep) {
      _Pragma("nounroll") for (unsigned spins = 0; spins < SPIN_CAP; ++spins) {
        if (__all(LDSV(unsigned, flag_off) == 1u)) break;
        __builtin_amdgcn_s_sleep(decltype(sleep)::value);
      }
    };
    wait_flags(L_ML + c * 16 + 8, IC(8));
    float wt;
    {
      const u32x4 e = LDSV(u32x4, L_ML + c * 16);
      const float mw = f(e[0]);
      float M = fmaxf(mw, dpp<ROR4>(mw)); M = fmaxf(M, dpp<XOR8>(M));
      float L = __builtin_amdgcn_exp2f((mw - M) * C2) * f(e[1]); L += dpp<ROR4>(L); L += dpp<XOR8>(L);
      publish(f32x2{M, L}, rws, lane < 4 ? (int)(WS_ML + lane * 16) : OOB, box, tag);
      unsigned touch = __builtin_amdgcn_raw_buffer_load_b32(MK_RSRC(gb ? p.wo[1] : p.wo[0], 4096 * 640 * 2), lane < 4 ? (h * 128 + lane * 32) * 1280 + blk0 * 256 : OOB, 0, NT);
      asm volatile("" :: "v"(touch));
      drain();
      f32x2 t[1];
      gather<1, 32>(t, rws, [&](int) { return WS_ML + ((lane >> 2) & 7) * WGBLK + (lane & 3) * 16; }, obox, tag);
      const float mj = t[0][0];
      float MM = fmaxf(mj, dpp<ROR4>(mj)); MM = fmaxf(MM, dpp<XOR8>(MM)); MM = x16_max(MM);
      float LL = __builtin_amdgcn_exp2f((mj - MM) * C2) * t[0][1]; LL += dpp<ROR4>(LL); LL += dpp<XOR8>(LL); LL = x16_sum(LL);
      wt = __builtin_amdgcn_exp2f((mw - MM) * C2) / LL;
    }
    auto share = [&](int r) {
      f32x2 o2 = {0.f, 0.f};
#pragma unroll
      for (int i = 0; i < 4; ++i) o2 += f(__builtin_amdgcn_readlane(u(wt), i * 4 + r)) * LDSV(f32x2, L_ACC + r * 2048 + i * 512 + lane * 8);
      return o2;
    };
    wait_flags(L_AF + (lane & 7) * 4, IC(8));
    {
      const f32x2 o0 = share(0), o1 = share(1);
      wait_flags(L_AF + 32 + (lane & 3) * 4, IC(4));
      publish(share(2), rws, WS_QG + lane * 16, box, tag + 1u);
      __builtin_amdgcn_s_sleep(20);
      publish(o0, rws, WS_PG + lane * 16, box, tag);
      publish(o1, rws, WS_PG + 1024 + lane * 16, box, tag);
    }
    wait_flags(L_AF + 48 + (lane & 3) * 4, IC(1));
    publish(share(3), rws, WS_QG + 1024 + lane * 16, box, tag + 1u);
    drain();
    LDSV(unsigned, L_OF) = 1u;
    wg_barrier();
#pragma unroll
    for (int blk = 0; blk < 2; ++blk) {
      if (blk == 1 && !two) break;
      const int src = L_RED + blk * 4096 + lane * 8;
      f32x2 sa = LDSV(f32x2, src), sb = LDSV(f32x2, src + 512);
#pragma unroll
      for (int w = 1; w < NW; ++w) { sa += LDSV(f32x2, src + w * 1024); sb += LDSV(f32x2, src + w * 1024 + 512); }
      publish(sa, rws, WS_YG + blk * 2048 + lane * 16, obox + (gb * 5 + blk0) * 2048, tag);
      publish(sb, rws, WS_YG + blk * 2048 + 1024 + lane * 16, obox + (gb * 5 + blk0) * 2048, tag);
    }
    if (ri < 80) {
      f32x2 r[4];
      gather<4, 1>(r, rws, [&](int k) { return yoff + 8 * k * OBLK; }, ysrc, tag);
      const f32x2 y = (r[0] + r[1]) + (r[2] + r[3]);
      float y0 = y[0], y1 = y[1];
      y0 += dpp<XOR8>(y0); y1 += dpp<XOR8>(y1);
      y0 = x32_sum(x16_sum(y0)); y1 = x32_sum(x16_sum(y1));
      __builtin_amdgcn_raw_buffer_store_b32(pack_bf16(y0, y1), MK_RSRC(p.out, 5120), lane < 8 ? lane * 4 : OOB, (ob * 1280 + rbr * 640 + cb * 16) * 2, SC1);
      if (bid == 5) __builtin_amdgcn_raw_buffer_store_b32(tag + 1u, rws, lane == 0 ? (int)WS_EPOCH : OOB, 0, SC1);
    }
  }
}

extern "C" void kernel_launch(void* const* d_in, const int* in_sizes, int n_in, void* d_out, int out_size, void* d_ws,
                              size_t ws_size, hipStream_t stream) {
  Params p;
  p.x[0] = d_in[0]; p.k[0] = d_in[1]; p.v[0] = d_in[2]; p.wq[0] = d_in[3]; p.wo[0] = d_in[4];
  p.x[1] = d_in[5]; p.k[1] = d_in[6]; p.v[1] = d_in[7]; p.wq[1] = d_in[8]; p.wo[1] = d_in[9];
  p.out = d_out; p.ws = d_ws;
  fused_decode_kernel<<<256, NTHR, LDS_BYTES, stream>>>(p);
}
```
